```python
import torch

def _softmax(x, axis=-1):
    return torch.softmax(x, dim=axis)
def _gelu(x):
    return torch.nn.functional.gelu(x, approximate="tanh")
def _silu(x):
    return torch.nn.functional.silu(x)
def _sigmoid(x):
    return torch.sigmoid(x)
def _layernorm(x, axis=-1, eps=1e-5):
    m = x.mean(dim=axis, keepdim=True)
    v = x.var(dim=axis, keepdim=True, unbiased=False)
    return (x - m) / torch.sqrt(v + eps)
def _scatter_add(tgt, idx, src, axis=0):
    return tgt.clone().index_add_(axis, idx, src)
def _segment_sum(src, seg, n):
    out = torch.zeros((n,) + src.shape[1:], dtype=src.dtype)
    return out.index_add_(0, seg, src)
def _pad(x, pw):
    flat = tuple(v for pair in reversed(pw) for v in pair)
    return torch.nn.functional.pad(x, flat)

kernel_name = 'concat_heads__mha_block_cached__gqa_block_cached__near_ridge'

def setup_inputs():

    _w = (lambda a: (lambda t: t.to(torch.bfloat16) if t.is_floating_point() else t)(torch.as_tensor(a)))
    def _A_setup():
        B, Sq, D = 16, 160, 512
        H, Sk, Dh = 32, 1024, 128
        return {
            "x": _w((lambda *s: torch.randn(*s, dtype=torch.bfloat16) * 0.8)(B, Sq, D)),
            "k_cache": _w((lambda *s: torch.randn(*s, dtype=torch.bfloat16) * 0.8)(B, H, Sk, Dh)),
            "v_cache": _w((lambda *s: torch.randn(*s, dtype=torch.bfloat16) * 0.05)(B, H, Sk, Dh)),
            "wq": _w((lambda *s: torch.randn(*s, dtype=torch.bfloat16) * 0.8)(D, H * Dh) * (D ** -0.5)),
            "wo": _w((lambda *s: torch.randn(*s, dtype=torch.bfloat16) * 0.05)(H * Dh, D) * ((H * Dh) ** -0.5)),
        }
    _A_in = _A_setup()

    def _B_setup():
        B, Sq, D = 16, 160, 512
        Hq, Hkv, Sk, Dh = 32, 32, 1024, 128
        assert Hq % Hkv == 0, (Hq, Hkv)
        return {
            "x": _w((lambda *s: torch.randn(*s, dtype=torch.bfloat16) * 0.8)(B, Sq, D)),
            "k_cache": _w((lambda *s: torch.randn(*s, dtype=torch.bfloat16) * 0.8)(B, Hkv, Sk, Dh)),
            "v_cache": _w((lambda *s: torch.randn(*s, dtype=torch.bfloat16) * 0.05)(B, Hkv, Sk, Dh)),
            "wq": _w((lambda *s: torch.randn(*s, dtype=torch.bfloat16) * 0.8)(D, Hq * Dh) * (D ** -0.5)),
            "wo": _w((lambda *s: torch.randn(*s, dtype=torch.bfloat16) * 0.05)(Hq * Dh, D) * ((Hq * Dh) ** -0.5)),
        }
    _B_in = _B_setup()

    return {
        "A_x": _A_in["x"],
        "A_k_cache": _A_in["k_cache"],
        "A_v_cache": _A_in["v_cache"],
        "A_wq": _A_in["wq"],
        "A_wo": _A_in["wo"],
        "B_x": _B_in["x"],
        "B_k_cache": _B_in["k_cache"],
        "B_v_cache": _B_in["v_cache"],
        "B_wq": _B_in["wq"],
        "B_wo": _B_in["wo"],
    }

def reference(A_x, A_k_cache, A_v_cache, A_wq, A_wo, B_x, B_k_cache, B_v_cache, B_wq, B_wo):
    def _A_ref(x, k_cache, v_cache, wq, wo):
        B, Sq, D = x.shape
        H, Dh = 32, 128
        q = (lambda x, *d: x.permute(*d))((x @ wq).reshape(B, Sq, H, Dh), 0, 2, 1, 3)
        scores = torch.einsum("bhqd,bhkd->bhqk", q, k_cache) * (Dh ** -0.5)
        p = _softmax(scores, axis=-1)
        o = torch.einsum("bhqk,bhkd->bhqd", p, v_cache)
        return (lambda x, *d: x.permute(*d))(o, 0, 2, 1, 3).reshape(B, Sq, H * Dh) @ wo

    def _B_ref(x, k_cache, v_cache, wq, wo):
        B, Sq, D = x.shape
        Hq, Dh = 32, 128
        Hkv = k_cache.shape[1]
        rep = Hq // Hkv
        q = (lambda x, *d: x.permute(*d))((x @ wq).reshape(B, Sq, Hq, Dh), 0, 2, 1, 3)
        k = (lambda x, n, axis: torch.repeat_interleave(x, n, dim=axis))(k_cache, rep, axis=1)
        v = (lambda x, n, axis: torch.repeat_interleave(x, n, dim=axis))(v_cache, rep, axis=1)
        scores = torch.einsum("bhqd,bhkd->bhqk", q, k) * (Dh ** -0.5)
        p = _softmax(scores, axis=-1)
        o = torch.einsum("bhqk,bhkd->bhqd", p, v)
        return (lambda x, *d: x.permute(*d))(o, 0, 2, 1, 3).reshape(B, Sq, Hq * Dh) @ wo

    a = _A_ref(A_x, A_k_cache, A_v_cache, A_wq, A_wo)
    b = _B_ref(B_x, B_k_cache, B_v_cache, B_wq, B_wo)
    return torch.concatenate([a, b], axis=-1)
```

```cpp
#include <hip/hip_runtime.h>
#include <hip/hip_bf16.h>
#include <utility>
#include <type_traits>

typedef __attribute__((ext_vector_type(8))) short s16x8;
typedef __attribute__((ext_vector_type(4))) short s16x4;
typedef __attribute__((ext_vector_type(4))) float f32x4;
typedef __attribute__((ext_vector_type(2))) float f32x2;
typedef __attribute__((ext_vector_type(2))) __bf16 bf16x2;
typedef __attribute__((ext_vector_type(4))) unsigned u32x4;
typedef __attribute__((address_space(3))) s16x4* lds_s16x4_ptr;
typedef __attribute__((address_space(3))) s16x8* lds_s16x8_ptr;
typedef __attribute__((address_space(3))) f32x4* lds_f32x4_ptr;

#define DI __device__ __forceinline__

constexpr int NB = 16, SQ = 160, DM = 512, NH = 32, SK = 1024, DH = 128;
constexpr int NS = 5;
constexpr int SLOT = 16384;
constexpr float QSCALE = 0.088388347648318440f * 1.4426950408889634f;
constexpr float THR = 8.0f;

DI unsigned cvtpk(float lo, float hi) { f32x2 v = {lo, hi}; bf16x2 b = __builtin_convertvector(v, bf16x2); return __builtin_bit_cast(unsigned, b); }
DI s16x8 pack8(f32x4 a, f32x4 b) { u32x4 w = {cvtpk(a[0], a[1]), cvtpk(a[2], a[3]), cvtpk(b[0], b[1]), cvtpk(b[2], b[3])}; return __builtin_bit_cast(s16x8, w); }
DI float max3f(float a, float b, float c) { float r; asm("v_max3_f32 %0, %1, %2, %3" : "=v"(r) : "v"(a), "v"(b), "v"(c)); return r; }
DI float max2f(float a, float b) { float r; asm("v_max_f32_e32 %0, %1, %2" : "=v"(r) : "v"(a), "v"(b)); return r; }
DI f32x4 mfma16(s16x8 a, s16x8 b, f32x4 c) { return __builtin_amdgcn_mfma_f32_16x16x32_bf16(a, b, c, 0, 0, 0); }
DI s16x8 lds_b128(unsigned addr) { return *(lds_s16x8_ptr)(addr); }
DI s16x4 lds_tr(unsigned addr) { return __builtin_amdgcn_ds_read_tr16_b64_v4i16((lds_s16x4_ptr)(addr)); }
DI s16x8 cat8(s16x4 a, s16x4 b) { return __builtin_shufflevector(a, b, 0, 1, 2, 3, 4, 5, 6, 7); }

DI void dma4(unsigned ldsaddr, unsigned v0, unsigned v1, unsigned v2, unsigned v3, const void* gbase) {
  asm volatile("s_mov_b32 m0, %0\n\ts_nop 0\n\t"
               "global_load_lds_dwordx4 %1, %5\n\t"
               "global_load_lds_dwordx4 %2, %5 offset:1024\n\t"
               "global_load_lds_dwordx4 %3, %5 offset:2048\n\t"
               "global_load_lds_dwordx4 %4, %5 offset:3072"
               :: "s"(ldsaddr), "v"(v0), "v"(v1), "v"(v2), "v"(v3), "s"(gbase) : "memory");
}
DI void dma1(unsigned ldsaddr, unsigned v0, const void* gbase) {
  asm volatile("s_mov_b32 m0, %0\n\ts_nop 0\n\t"
               "global_load_lds_dwordx4 %1, %2"
               :: "s"(ldsaddr), "v"(v0), "s"(gbase) : "memory");
}
template <int OFF> DI s16x8 gload16(unsigned voff, const void* gbase) {
  s16x8 r; asm volatile("global_load_dwordx4 %0, %1, %2 offset:%3" : "=v"(r) : "v"(voff), "s"(gbase), "i"(OFF) : "memory"); return r;
}
#define WAITV(n) asm volatile("s_waitcnt vmcnt(" #n ")" ::: "memory")
#define BARRIER() asm volatile("s_barrier" ::: "memory")
#define PIN5(a) asm volatile("" : "+v"(a[0]), "+v"(a[1]), "+v"(a[2]), "+v"(a[3]), "+v"(a[4]))

#define ACLOB "a0","a1","a2","a3","a4","a5","a6","a7","a8","a9","a10","a11","a12","a13","a14","a15","a16","a17","a18","a19","a20","a21","a22","a23","a24","a25","a26","a27","a28","a29","a30","a31","a32","a33","a34","a35","a36","a37","a38","a39","a40","a41","a42","a43","a44","a45","a46","a47","a48","a49","a50","a51","a52","a53","a54","a55","a56","a57","a58","a59","a60","a61","a62","a63","a64","a65","a66","a67","a68","a69","a70","a71","a72","a73","a74","a75","a76","a77","a78","a79","a80","a81","a82","a83","a84","a85","a86","a87","a88","a89","a90","a91","a92","a93","a94","a95","a96","a97","a98","a99","a100","a101","a102","a103","a104","a105","a106","a107","a108","a109","a110","a111","a112","a113","a114","a115","a116","a117","a118","a119","a120","a121","a122","a123","a124","a125","a126","a127","a128","a129","a130","a131","a132","a133","a134","a135","a136","a137","a138","a139","a140","a141","a142","a143","a144","a145","a146","a147","a148","a149","a150","a151","a152","a153","a154","a155","a156","a157","a158","a159","a160","a161","a162","a163","a164","a165","a166","a167","a168","a169","a170","a171","a172","a173","a174","a175","a176","a177","a178","a179","a180","a181","a182","a183","a184","a185","a186","a187","a188","a189","a190","a191","a192","a193","a194","a195","a196","a197","a198","a199","a200","a201","a202","a203","a204","a205","a206","a207","a208","a209","a210","a211","a212","a213","a214","a215","a216","a217","a218","a219","a220","a221","a222","a223","a224","a225","a226","a227","a228","a229","a230","a231","a232","a233","a234","a235","a236","a237","a238","a239"
#define SB() __builtin_amdgcn_sched_barrier(0)
template <int... I, class F> DI void sfor_impl(std::integer_sequence<int, I...>, F&& f) { (f(std::integral_constant<int, I>{}), ...); }
template <int N, class F> DI void sfor(F&& f) { sfor_impl(std::make_integer_sequence<int, N>{}, f); }
constexpr int AO(int rb, int db) { return (rb * 8 + db) * 4; }
constexpr int AQ(int rb, int ks) { return 160 + (rb * 4 + ks) * 4; }
template <int A> DI void mfma_acc(s16x8 a, s16x8 b) {
  asm volatile("v_mfma_f32_16x16x32_bf16 a[%c2:%c3], %0, %1, a[%c2:%c3]" :: "v"(a), "v"(b), "i"(A), "i"(A + 3) : ACLOB);
}
template <int A> DI void mfma_qk0(f32x4& S, s16x8 kf, const f32x4& nm) {
  asm volatile("v_mfma_f32_16x16x32_bf16 %0, %1, a[%c3:%c4], %2" : "=&v"(S) : "v"(kf), "v"(nm), "i"(A), "i"(A + 3));
}
template <int A> DI void mfma_qk(f32x4& S, s16x8 kf) {
  asm volatile("v_mfma_f32_16x16x32_bf16 %0, %1, a[%c2:%c3], %0" : "+v"(S) : "v"(kf), "i"(A), "i"(A + 3));
}
DI void mfma_v(f32x4& D, s16x8 a, s16x8 b) { asm volatile("v_mfma_f32_16x16x32_bf16 %0, %1, %2, %0" : "+v"(D) : "v"(a), "v"(b)); }
template <int A> DI float acc_rd() { float r; asm volatile("v_accvgpr_read_b32 %0, a[%c1]" : "=v"(r) : "i"(A)); return r; }
template <int A> DI void acc_wr(unsigned v) { asm volatile("v_accvgpr_write_b32 a[%c1], %0" :: "v"(v), "i"(A) : ACLOB); }
template <int A> DI void acc_zero() { asm volatile("v_accvgpr_write_b32 a[%c0], 0" :: "i"(A) : ACLOB); }
template <int A> DI void acc_scale(float f) { float t; asm volatile("v_accvgpr_read_b32 %0, a[%c2]\n\tv_mul_f32 %0, %0, %1\n\tv_accvgpr_write_b32 a[%c2], %0" : "=&v"(t) : "v"(f), "i"(A) : ACLOB); }
template <int OFF> DI void dma1o(unsigned ldsaddr, unsigned v0, const void* gbase) {
  asm volatile("s_mov_b32 m0, %0\n\ts_nop 0\n\t"
               "global_load_lds_dwordx4 %1, %2 offset:%3"
               :: "s"(ldsaddr), "v"(v0), "s"(gbase), "i"(OFF) : "memory");
}
constexpr int NSL = 10, TILE = 8192;

__global__ __launch_bounds__(128, 1)
void attn_kernel(const __hip_bfloat16* __restrict__ xA, const __hip_bfloat16* __restrict__ kA, const __hip_bfloat16* __restrict__ vA, const __hip_bfloat16* __restrict__ wqA,
                 const __hip_bfloat16* __restrict__ xB, const __hip_bfloat16* __restrict__ kB, const __hip_bfloat16* __restrict__ vB, const __hip_bfloat16* __restrict__ wqB,
                 __hip_bfloat16* __restrict__ ows) {
  __shared__ __attribute__((aligned(16))) char smem[NSL * TILE];
  const unsigned lds0 = (unsigned)(size_t)smem;
  const int tid = threadIdx.x, lane = tid & 63;
  const int w = __builtin_amdgcn_readfirstlane(tid >> 6);
  const int li = lane & 15, g = lane >> 4, q4 = (lane >> 2) & 3, p4 = lane & 3;

  const int idx = blockIdx.x, xcd = idx & 7, j0 = idx >> 3, blk = j0 >> 6, jj = j0 & 63;
  const int b = (xcd >> 2) * 8 + (jj >> 3), h = (xcd & 3) * 8 + (jj & 7);
  const char* xg = (const char*)(blk ? xB : xA) + (size_t)b * SQ * DM * 2;
  const char* wqg = (const char*)(blk ? wqB : wqA) + (size_t)h * DH * 2 + (size_t)w * 16 * 8192;
  const char* kg = (const char*)(blk ? kB : kA) + ((size_t)b * NH + h) * SK * DH * 2 + w * 4096;
  const char* vg = (const char*)(blk ? vB : vA) + ((size_t)b * NH + h) * SK * DH * 2 + w * 4096;
  const unsigned ldsw = lds0 + w * 4096;

  unsigned voffK[4], voffV[2], voffW[2];
#pragma unroll
  for (int n = 0; n < 4; ++n) voffK[n] = g * 256 + (((li ^ g) ^ (4 * n)) * 16);
#pragma unroll
  for (int n = 0; n < 2; ++n) voffV[n] = g * 256 + ((li ^ (g << 1) ^ (n << 3)) * 16);
#pragma unroll
  for (int n = 0; n < 2; ++n) voffW[n] = g * 8192 + ((li ^ ((g | (n << 2)) << 1)) * 16);
  unsigned kofs[4], wofs[4];
#pragma unroll
  for (int ks = 0; ks < 4; ++ks) kofs[ks] = lds0 + li * 256 + (((4 * ks + g) ^ li) * 16);
  const unsigned r7 = 4 * (g & 1) + q4;
  const unsigned vofs = lds0 + (4 * g + q4) * 256 + r7 * 32 + (p4 >> 1) * 16 + (p4 & 1) * 8;
#pragma unroll
  for (int t = 0; t < 4; ++t) wofs[t] = lds0 + (8 * g + q4) * 256 + (((4 * t + p4) ^ (2 * q4 | ((g & 1) << 3))) * 16);
  unsigned xoff[5];
#pragma unroll
  for (int rb = 0; rb < 5; ++rb) xoff[rb] = (80 * w + 16 * rb + li) * 1024 + g * 16;

  auto issueW = [&](int n, int slot) {
    const char* gb = wqg + (size_t)(32 * n) * 8192;
#pragma unroll
    for (int pc = 0; pc < 4; ++pc) dma1(ldsw + slot * TILE + pc * 1024, voffW[(pc >> 1) & 1], gb + (size_t)pc * 32768);
  };
  auto issueK = [&](int kt, int slot) { dma4(ldsw + slot * TILE, voffK[0], voffK[1], voffK[2], voffK[3], kg + (size_t)kt * 8192); };
  auto issueV = [&](int vt, int slot) { dma4(ldsw + slot * TILE, voffV[0], voffV[1], voffV[0], voffV[1], vg + (size_t)vt * 8192); };
  auto issueT = [&](int T) {
    if (T < 16) issueW(T, T % NSL); else if (T == 16) issueK(0, T % NSL);
    else if (T & 1) issueK((T - 15) / 2, T % NSL); else issueV((T - 18) / 2, T % NSL);
  };

  sfor<160>([&](auto I) { acc_zero<decltype(I)::value>(); });

  s16x8 xf[4][5];
#define XLOAD(n) do { _Pragma("unroll") for (int rb = 0; rb < 5; ++rb) xf[(n) & 3][rb] = gload16<64 * (n)>(xoff[rb], xg); } while (0)
  XLOAD(0); issueT(0); issueT(1); XLOAD(1); issueT(2); XLOAD(2);
  issueT(3); issueT(4); issueT(5); issueT(6); issueT(7); issueT(8);
#define QSTEP(n, WAITN) do { \
    WAITV(WAITN); PIN5(xf[(n) & 3]); BARRIER(); \
    s16x8 wf[8]; \
    _Pragma("unroll") for (int t = 0; t < 4; ++t) { _Pragma("unroll") for (int u = 0; u < 2; ++u) { \
        const unsigned a = wofs[t] + ((n) % NSL) * TILE + u * 8; wf[2 * t + u] = cat8(lds_tr(a), lds_tr(a + 1024)); } } \
    SB(); \
    issueT((n) + 9); \
    if constexpr ((n) + 3 < 16) XLOAD((n) + 3); \
    SB(); \
    sfor<8>([&](auto D) { sfor<5>([&](auto R) { mfma_acc<AO(decltype(R)::value, decltype(D)::value)>(wf[decltype(D)::value], xf[(n) & 3][decltype(R)::value]); }); }); \
  } while (0)
  QSTEP(0, 42); QSTEP(1, 42); QSTEP(2, 42); QSTEP(3, 18); QSTEP(4, 18); QSTEP(5, 18); QSTEP(6, 18); QSTEP(7, 18);
  QSTEP(8, 18); QSTEP(9, 18); QSTEP(10, 18); QSTEP(11, 18); QSTEP(12, 18); QSTEP(13, 18); QSTEP(14, 13); QSTEP(15, 8);
#undef QSTEP
#undef XLOAD
  asm volatile("s_nop 7\n\ts_nop 7" ::: "memory");
  sfor<5>([&](auto R) { sfor<4>([&](auto Tt) {
    constexpr int rb = decltype(R)::value, t = decltype(Tt)::value;
    float v[8];
    sfor<8>([&](auto E) { v[decltype(E)::value] = acc_rd<AO(rb, 2 * t) + decltype(E)::value>() * QSCALE; });
    sfor<4>([&](auto E) { constexpr int e = decltype(E)::value; acc_wr<AQ(rb, t) + e>(cvtpk(v[2 * e], v[2 * e + 1])); });
  }); });
  sfor<160>([&](auto I) { acc_zero<decltype(I)::value>(); });

  f32x4 L[5], NM[5], SA[5][2], SBB[5][2];
  unsigned Pw[5][4];
  s16x8 ONES = {0x3F80, 0x3F80, 0x3F80, 0x3F80, 0x3F80, 0x3F80, 0x3F80, 0x3F80};
  asm volatile("" : "+v"(ONES));
#pragma unroll
  for (int rb = 0; rb < 5; ++rb) { L[rb] = f32x4{0.f, 0.f, 0.f, 0.f}; NM[rb] = f32x4{0.f, 0.f, 0.f, 0.f}; }

  auto rowmax = [&](const f32x4 (&Sr)[2]) {
    float t = max3f(Sr[0][0], Sr[0][1], Sr[0][2]);
    t = max3f(t, Sr[0][3], Sr[1][0]); t = max3f(t, Sr[1][1], Sr[1][2]); t = max2f(t, Sr[1][3]);
    return t;
  };
  auto xlane_max = [&](float t) { t = fmaxf(t, __shfl_xor(t, 16)); t = fmaxf(t, __shfl_xor(t, 32)); return t; };
  auto sm_item = [&](f32x4 (&S)[5][2], int rb, int k) {
    if (k < 8) S[rb][k >> 2][k & 3] = __builtin_amdgcn_exp2f(S[rb][k >> 2][k & 3]);
    else { const int c = k - 8; Pw[rb][c] = cvtpk(S[rb][c >> 1][(c & 1) * 2], S[rb][c >> 1][(c & 1) * 2 + 1]); }
  };
  auto Pf = [&](int rb) { u32x4 v = {Pw[rb][0], Pw[rb][1], Pw[rb][2], Pw[rb][3]}; return __builtin_bit_cast(s16x8, v); };

  {
    WAITV(32); BARRIER();
    s16x8 kf[2][4];
#pragma unroll
    for (int kb = 0; kb < 2; ++kb)
#pragma unroll
      for (int ks = 0; ks < 4; ++ks) kf[kb][ks] = lds_b128(kofs[ks] + (16 % NSL) * TILE + kb * 4096);
    asm volatile("s_nop 1" ::: "memory");
    sfor<8>([&](auto G8) { sfor<5>([&](auto R) {
      constexpr int kb = decltype(G8)::value >> 2, ks = decltype(G8)::value & 3, rb = decltype(R)::value;
      if constexpr (ks == 0) mfma_qk0<AQ(rb, ks)>(SA[rb][kb], kf[kb][ks], NM[rb]); else mfma_qk<AQ(rb, ks)>(SA[rb][kb], kf[kb][ks]);
    }); });
    asm volatile("s_nop 7\n\ts_nop 7" : "+v"(SA[0][0]), "+v"(SA[0][1]), "+v"(SA[1][0]), "+v"(SA[1][1]), "+v"(SA[2][0]), "+v"(SA[2][1]), "+v"(SA[3][0]), "+v"(SA[3][1]), "+v"(SA[4][0]), "+v"(SA[4][1]));
#pragma unroll
    for (int rb = 0; rb < 5; ++rb) {
      const float m = xlane_max(rowmax(SA[rb]));
      SA[rb][0] -= m; SA[rb][1] -= m; NM[rb] -= m;
    }
  }
  int ks_slot = 17 % NSL;
  auto aiter = [&](int j, f32x4 (&SC)[5][2], f32x4 (&SN)[5][2], auto LASTC) {
    constexpr bool LAST = decltype(LASTC)::value;
    const int vs_slot = ks_slot == NSL - 1 ? 0 : ks_slot + 1;
    const int fk = ks_slot >= 2 ? ks_slot - 2 : ks_slot + 8;
    if (j < 28) WAITV(24); else WAITV(0);
    BARRIER();
    s16x8 kf[2][4], vf[8];
    if constexpr (!LAST) {
#pragma unroll
      for (int kb = 0; kb < 2; ++kb)
#pragma unroll
        for (int ks = 0; ks < 4; ++ks) kf[kb][ks] = lds_b128(kofs[ks] + ks_slot * TILE + kb * 4096);
    }
    {
      const unsigned va = vofs + vs_slot * TILE;
#pragma unroll
      for (int db = 0; db < 8; ++db) { const unsigned a = va ^ (db << 5); vf[db] = cat8(lds_tr(a), lds_tr(a + 4096)); }
    }
    SB();
    const bool doK = j + 5 < 32, doV = j + 4 < 32;
    const char* kgn = kg + (size_t)(j + 5) * 8192; const char* vgn = vg + (size_t)(j + 4) * 8192;
    const unsigned lk = ldsw + fk * TILE, lv = ldsw + (fk == NSL - 1 ? 0 : fk + 1) * TILE;
    if constexpr (!LAST) {
#pragma unroll
      for (int k = 0; k < 24; ++k) sm_item(SC, k / 12, k % 12);
      SB();
      sfor<40>([&](auto M) {
        constexpr int m = decltype(M)::value, g8 = m / 5, rb = m % 5, kb = g8 >> 2, ks = g8 & 3;
        if constexpr (ks == 0) mfma_qk0<AQ(rb, ks)>(SN[rb][kb], kf[kb][ks], NM[rb]); else mfma_qk<AQ(rb, ks)>(SN[rb][kb], kf[kb][ks]);
        if constexpr (m < 36) sm_item(SC, 2 + m / 12, m % 12);
        if constexpr (m % 5 == 2) {
          constexpr int pc = m / 5;
          if constexpr (pc < 4) { if (doK) dma1o<pc * 1024>(lk, voffK[pc], kgn); }
          else { if (doV) dma1o<(pc - 4) * 1024>(lv, voffV[pc & 1], vgn); }
        }
        SB();
      });
    } else {
#pragma unroll
      for (int k = 0; k < 60; ++k) sm_item(SC, k / 12, k % 12);
      SB();
    }
    float tm = 0.f;
    asm volatile("s_nop 1" ::: "memory");
    sfor<45>([&](auto M) {
      constexpr int m = decltype(M)::value, rb = m / 9, db = m % 9;
      if constexpr (db < 8) mfma_acc<AO(rb, db)>(vf[db], Pf(rb)); else mfma_v(L[rb], ONES, Pf(rb));
      if constexpr (!LAST && m < 20) {
        constexpr int kb = m / 10, r2 = (m % 10) >> 1, hf = m & 1;
        if constexpr (m == 0) tm = max2f(SN[0][0][0], SN[0][0][1]); else tm = max3f(tm, SN[r2][kb][2 * hf], SN[r2][kb][2 * hf + 1]);
      }
      SB();
    });
    if constexpr (!LAST) {
      if (__builtin_expect(__any(tm > THR), 0)) {
        asm volatile("s_nop 7\n\ts_nop 7" : "+v"(L[0]), "+v"(L[1]), "+v"(L[2]), "+v"(L[3]), "+v"(L[4]));
        sfor<5>([&](auto R) {
          constexpr int rb = decltype(R)::value;
          const float dl = fmaxf(xlane_max(rowmax(SN[rb])), 0.f);
          const float al = __builtin_amdgcn_exp2f(-dl);
          SN[rb][0] -= dl; SN[rb][1] -= dl; NM[rb] -= dl; L[rb] *= al;
          sfor<32>([&](auto E) { acc_scale<AO(rb, 0) + decltype(E)::value>(al); });
        });
      }
    }
    ks_slot = ks_slot + 2 >= NSL ? ks_slot + 2 - NSL : ks_slot + 2;
  };
  for (int j = 0; j < 30; j += 2) {
    aiter(j, SA, SBB, std::false_type{});
    aiter(j + 1, SBB, SA, std::false_type{});
  }
  aiter(30, SA, SBB, std::false_type{});
  aiter(31, SBB, SA, std::true_type{});

  asm volatile("s_nop 7\n\ts_nop 7" : "+v"(L[0]), "+v"(L[1]), "+v"(L[2]), "+v"(L[3]), "+v"(L[4]));
  char* ob = (char*)ows + ((((size_t)(blk * NB + b) * 10 + 5 * w) * NH + h) * 4) * 1024 + lane * 16;
  sfor<5>([&](auto R) {
    constexpr int rb = decltype(R)::value;
    const float il = __builtin_amdgcn_rcpf(L[rb][0]);
    sfor<4>([&](auto Tt) {
      constexpr int t = decltype(Tt)::value;
      float v[8];
      sfor<8>([&](auto E) { v[decltype(E)::value] = acc_rd<AO(rb, 2 * t) + decltype(E)::value>() * il; });
      u32x4 o = {cvtpk(v[0], v[1]), cvtpk(v[2], v[3]), cvtpk(v[4], v[5]), cvtpk(v[6], v[7])};
      *(u32x4*)(ob + (size_t)rb * (NH * 4 * 1024) + t * 1024) = o;
    });
  });
}

constexpr int K2_NBUF = 4;
constexpr int K2_LDS = 3 * 4 * 10240 * 1;
__global__ __launch_bounds__(256, 1)
void oproj_kernel(const __hip_bfloat16* __restrict__ ows, const __hip_bfloat16* __restrict__ woA, const __hip_bfloat16* __restrict__ woB,
                  __hip_bfloat16* __restrict__ out) {
  __shared__ __attribute__((aligned(16))) char smem[K2_LDS];
  const unsigned lds0 = (unsigned)(size_t)smem;
  const int tid = threadIdx.x, lane = tid & 63;
  const int w = __builtin_amdgcn_readfirstlane(tid >> 6);
  const int li = lane & 15, g = lane >> 4, q4 = (lane >> 2) & 3, p4 = lane & 3;
  const int idx = blockIdx.x, xcd = idx & 7, r = idx >> 3;
  const int nt = r & 7, bb = (r >> 3) * 8 + xcd;
  const int blk = bb >> 4;
  const char* og = (const char*)ows + (size_t)bb * 10 * 128 * 1024 + (size_t)w * 32 * 1024;
  const char* wog = (const char*)(blk ? woB : woA) + (size_t)(w * 1024) * 1024 + nt * 128;
  const unsigned rowl = lane >> 3, pos = lane & 7;
  const unsigned voffw = rowl * 1024 + (((((pos >> 1) ^ ((rowl >> 1) & 3)) << 1) | (pos & 1)) * 16);
  const unsigned wbase = lds0 + w * (K2_NBUF * 4096);
  const unsigned rr = 4 * g + q4;
  const unsigned trb = wbase + rr * 128 + ((rr >> 1) & 3) * 32 + p4 * 8;
  const unsigned ovoff = lane * 16;

  auto issueWo = [&](int sl, int ks) {
    const unsigned lb = wbase + (sl & (K2_NBUF - 1)) * 4096;
    const char* gb = wog + (size_t)ks * 32 * 1024;
#pragma unroll
    for (int pc = 0; pc < 4; ++pc) dma1(lb + pc * 1024, voffw, gb + pc * 8192);
  };
  f32x4 acc[10][4];
#pragma unroll
  for (int rb = 0; rb < 10; ++rb)
#pragma unroll
    for (int nb = 0; nb < 4; ++nb) acc[rb][nb] = f32x4{0.f, 0.f, 0.f, 0.f};
  s16x8 of[2][10];
  auto loadO = [&](s16x8 (&dst)[10], int ks) {
#pragma unroll
    for (int rb = 0; rb < 10; ++rb) dst[rb] = gload16<0>(ovoff, og + (size_t)rb * 131072 + (size_t)ks * 1024);
  };
#define PIN10(a) asm volatile("" : "+v"(a[0]), "+v"(a[1]), "+v"(a[2]), "+v"(a[3]), "+v"(a[4]), "+v"(a[5]), "+v"(a[6]), "+v"(a[7]), "+v"(a[8]), "+v"(a[9]))
  auto compute = [&](s16x8 (&src)[10], int ks) {
    const unsigned a0 = trb + (ks & (K2_NBUF - 1)) * 4096;
#pragma unroll
    for (int nb = 0; nb < 4; ++nb) {
      const unsigned a = a0 ^ (nb << 5);
      const s16x8 wf = cat8(lds_tr(a), lds_tr(a + 2048));
#pragma unroll
      for (int rb = 0; rb < 10; ++rb) acc[rb][nb] = mfma16(wf, src[rb], acc[rb][nb]);
    }
  };
  issueWo(0, 0); issueWo(1, 1); loadO(of[0], 0);
  for (int ks = 0; ks < 32; ks += 2) {
    issueWo(ks + 2, ks + 2 < 32 ? ks + 2 : 31);
    loadO(of[1], ks + 1);
    WAITV(14); PIN10(of[0]);
    compute(of[0], ks);
    issueWo(ks + 3, ks + 3 < 32 ? ks + 3 : 31);
    loadO(of[0], ks + 2 < 32 ? ks + 2 : 31);
    WAITV(14); PIN10(of[1]);
    compute(of[1], ks + 1);
  }
  WAITV(0);
  __syncthreads();
#pragma unroll
  for (int nb = 0; nb < 4; ++nb) {
    if (nb != w) {
      const int sl = (w - nb - 1) & 3;
#pragma unroll
      for (int rb = 0; rb < 10; ++rb) *(lds_f32x4_ptr)(lds0 + ((nb * 3 + sl) * 10 + rb) * 1024 + lane * 16) = acc[rb][nb];
    }
  }
  __syncthreads();
  char* outp = (char*)out + (size_t)(bb & 15) * SQ * 2048 + blk * 1024 + nt * 128 + w * 32 + g * 8;
#pragma unroll
  for (int rb = 0; rb < 10; ++rb) {
    f32x4 s;
    if (w == 0) s = acc[rb][0]; else if (w == 1) s = acc[rb][1]; else if (w == 2) s = acc[rb][2]; else s = acc[rb][3];
#pragma unroll
    for (int sl = 0; sl < 3; ++sl) s += *(lds_f32x4_ptr)(lds0 + ((w * 3 + sl) * 10 + rb) * 1024 + lane * 16);
    uint2 o2; o2.x = cvtpk(s[0], s[1]); o2.y = cvtpk(s[2], s[3]);
    *(uint2*)(outp + (size_t)(16 * rb + li) * 2048) = o2;
  }
}

extern "C" void kernel_launch(void* const* d_in, const int* in_sizes, int n_in,
                              void* d_out, int out_size, void* d_ws, size_t ws_size,
                              hipStream_t stream) {
  const __hip_bfloat16* xA = (const __hip_bfloat16*)d_in[0];
  const __hip_bfloat16* kA = (const __hip_bfloat16*)d_in[1];
  const __hip_bfloat16* vA = (const __hip_bfloat16*)d_in[2];
  const __hip_bfloat16* wqA = (const __hip_bfloat16*)d_in[3];
  const __hip_bfloat16* woA = (const __hip_bfloat16*)d_in[4];
  const __hip_bfloat16* xB = (const __hip_bfloat16*)d_in[5];
  const __hip_bfloat16* kB = (const __hip_bfloat16*)d_in[6];
  const __hip_bfloat16* vB = (const __hip_bfloat16*)d_in[7];
  const __hip_bfloat16* wqB = (const __hip_bfloat16*)d_in[8];
  const __hip_bfloat16* woB = (const __hip_bfloat16*)d_in[9];
  __hip_bfloat16* ows = (__hip_bfloat16*)d_ws;
  attn_kernel<<<1024, 128, 0, stream>>>(xA, kA, vA, wqA, xB, kB, vB, wqB, ows);
  oproj_kernel<<<256, 256, 0, stream>>>(ows, woA, woB, (__hip_bfloat16*)d_out);
}
```

```cpp
#include <hip/hip_runtime.h>
#include <hip/hip_bf16.h>
#include <utility>
#include <type_traits>

typedef __attribute__((ext_vector_type(8))) short s16x8;
typedef __attribute__((ext_vector_type(4))) short s16x4;
typedef __attribute__((ext_vector_type(4))) float f32x4;
typedef __attribute__((ext_vector_type(2))) float f32x2;
typedef __attribute__((ext_vector_type(2))) __bf16 bf16x2;
typedef __attribute__((ext_vector_type(4))) unsigned u32x4;
typedef __attribute__((address_space(3))) s16x4* lds_s16x4_ptr;
typedef __attribute__((address_space(3))) s16x8* lds_s16x8_ptr;
typedef __attribute__((address_space(3))) f32x4* lds_f32x4_ptr;

#define DI __device__ __forceinline__

constexpr int NB = 16, SQ = 160, DM = 512, NH = 32, SK = 1024, DH = 128;
constexpr int NS = 5;
constexpr int SLOT = 16384;
constexpr float QSCALE = 0.088388347648318440f * 1.4426950408889634f;
constexpr float THR = 8.0f;

DI unsigned cvtpk(float lo, float hi) { f32x2 v = {lo, hi}; bf16x2 b = __builtin_convertvector(v, bf16x2); return __builtin_bit_cast(unsigned, b); }
DI s16x8 pack8(f32x4 a, f32x4 b) { u32x4 w = {cvtpk(a[0], a[1]), cvtpk(a[2], a[3]), cvtpk(b[0], b[1]), cvtpk(b[2], b[3])}; return __builtin_bit_cast(s16x8, w); }
DI float max3f(float a, float b, float c) { float r; asm("v_max3_f32 %0, %1, %2, %3" : "=v"(r) : "v"(a), "v"(b), "v"(c)); return r; }
DI float max2f(float a, float b) { float r; asm("v_max_f32_e32 %0, %1, %2" : "=v"(r) : "v"(a), "v"(b)); return r; }
DI f32x4 mfma16(s16x8 a, s16x8 b, f32x4 c) { return __builtin_amdgcn_mfma_f32_16x16x32_bf16(a, b, c, 0, 0, 0); }
DI s16x8 lds_b128(unsigned addr) { return *(lds_s16x8_ptr)(addr); }
DI s16x4 lds_tr(unsigned addr) { return __builtin_amdgcn_ds_read_tr16_b64_v4i16((lds_s16x4_ptr)(addr)); }
DI s16x8 cat8(s16x4 a, s16x4 b) { return __builtin_shufflevector(a, b, 0, 1, 2, 3, 4, 5, 6, 7); }

DI void dma4(unsigned ldsaddr, unsigned v0, unsigned v1, unsigned v2, unsigned v3, const void* gbase) {
  asm volatile("s_mov_b32 m0, %0\n\ts_nop 0\n\t"
               "global_load_lds_dwordx4 %1, %5 nt\n\t"
               "global_load_lds_dwordx4 %2, %5 offset:1024 nt\n\t"
               "global_load_lds_dwordx4 %3, %5 offset:2048 nt\n\t"
               "global_load_lds_dwordx4 %4, %5 offset:3072 nt"
               :: "s"(ldsaddr), "v"(v0), "v"(v1), "v"(v2), "v"(v3), "s"(gbase) : "memory");
}
DI void dma1(unsigned ldsaddr, unsigned v0, const void* gbase) {
  asm volatile("s_mov_b32 m0, %0\n\ts_nop 0\n\t"
               "global_load_lds_dwordx4 %1, %2"
               :: "s"(ldsaddr), "v"(v0), "s"(gbase) : "memory");
}
template <int OFF> DI s16x8 gload16(unsigned voff, const void* gbase) {
  s16x8 r; asm volatile("global_load_dwordx4 %0, %1, %2 offset:%3" : "=v"(r) : "v"(voff), "s"(gbase), "i"(OFF) : "memory"); return r;
}
#define WAITV(n) asm volatile("s_waitcnt vmcnt(" #n ")" ::: "memory")
#define BARRIER() asm volatile("s_barrier" ::: "memory")
#define PIN5(a) asm volatile("" : "+v"(a[0]), "+v"(a[1]), "+v"(a[2]), "+v"(a[3]), "+v"(a[4]))

#define ACLOB "a0","a1","a2","a3","a4","a5","a6","a7","a8","a9","a10","a11","a12","a13","a14","a15","a16","a17","a18","a19","a20","a21","a22","a23","a24","a25","a26","a27","a28","a29","a30","a31","a32","a33","a34","a35","a36","a37","a38","a39","a40","a41","a42","a43","a44","a45","a46","a47","a48","a49","a50","a51","a52","a53","a54","a55","a56","a57","a58","a59","a60","a61","a62","a63","a64","a65","a66","a67","a68","a69","a70","a71","a72","a73","a74","a75","a76","a77","a78","a79","a80","a81","a82","a83","a84","a85","a86","a87","a88","a89","a90","a91","a92","a93","a94","a95","a96","a97","a98","a99","a100","a101","a102","a103","a104","a105","a106","a107","a108","a109","a110","a111","a112","a113","a114","a115","a116","a117","a118","a119","a120","a121","a122","a123","a124","a125","a126","a127","a128","a129","a130","a131","a132","a133","a134","a135","a136","a137","a138","a139","a140","a141","a142","a143","a144","a145","a146","a147","a148","a149","a150","a151","a152","a153","a154","a155","a156","a157","a158","a159","a160","a161","a162","a163","a164","a165","a166","a167","a168","a169","a170","a171","a172","a173","a174","a175","a176","a177","a178","a179","a180","a181","a182","a183","a184","a185","a186","a187","a188","a189","a190","a191","a192","a193","a194","a195","a196","a197","a198","a199","a200","a201","a202","a203","a204","a205","a206","a207","a208","a209","a210","a211","a212","a213","a214","a215","a216","a217","a218","a219","a220","a221","a222","a223","a224","a225","a226","a227","a228","a229","a230","a231","a232","a233","a234","a235","a236","a237","a238","a239"
#define SB() __builtin_amdgcn_sched_barrier(0)
template <int... I, class F> DI void sfor_impl(std::integer_sequence<int, I...>, F&& f) { (f(std::integral_constant<int, I>{}), ...); }
template <int N, class F> DI void sfor(F&& f) { sfor_impl(std::make_integer_sequence<int, N>{}, f); }
template <int N> using IC = std::integral_constant<int, N>;
constexpr int AO(int rb, int db) { return (rb * 8 + db) * 4; }
constexpr int AQ(int rb, int ks) { return 160 + (rb * 4 + ks) * 4; }
template <int A> DI void mfma_acc(s16x8 a, s16x8 b) {
  asm volatile("v_mfma_f32_16x16x32_bf16 a[%c2:%c3], %0, %1, a[%c2:%c3]" :: "v"(a), "v"(b), "i"(A), "i"(A + 3));
}
template <int A> DI void mfma_qk0(f32x4& S, s16x8 kf, const f32x4& nm) {
  asm volatile("v_mfma_f32_16x16x32_bf16 %0, %1, a[%c3:%c4], %2" : "=&v"(S) : "v"(kf), "v"(nm), "i"(A), "i"(A + 3));
}
template <int A> DI void mfma_qk(f32x4& S, s16x8 kf) {
  asm volatile("v_mfma_f32_16x16x32_bf16 %0, %1, a[%c2:%c3], %0" : "+v"(S) : "v"(kf), "i"(A), "i"(A + 3));
}
DI void mfma_v(f32x4& D, s16x8 a, s16x8 b) { asm volatile("v_mfma_f32_16x16x32_bf16 %0, %1, %2, %0" : "+v"(D) : "v"(a), "v"(b)); }
template <int A> DI float acc_rd() { float r; asm volatile("v_accvgpr_read_b32 %0, a[%c1]" : "=v"(r) : "i"(A)); return r; }
template <int A> DI void acc_wr(unsigned v) { asm volatile("v_accvgpr_write_b32 a[%c1], %0" :: "v"(v), "i"(A)); }
template <int A> DI void acc_zero4() { asm volatile("v_accvgpr_write_b32 a[%c0], 0\n\tv_accvgpr_write_b32 a[%c1], 0\n\tv_accvgpr_write_b32 a[%c2], 0\n\tv_accvgpr_write_b32 a[%c3], 0" :: "i"(A), "i"(A + 1), "i"(A + 2), "i"(A + 3)); }
template <int A> DI void acc_scale(float f) { float t; asm volatile("v_accvgpr_read_b32 %0, a[%c2]\n\tv_mul_f32 %0, %0, %1\n\tv_accvgpr_write_b32 a[%c2], %0" : "=&v"(t) : "v"(f), "i"(A)); }
template <int LOFF, int OFF> DI void dma_nt_m0(unsigned ldsw, unsigned v0, const void* gbase) {
  asm volatile("s_add_u32 m0, %0, %3\n\ts_nop 0\n\tglobal_load_lds_dwordx4 %1, %2 offset:%4 nt" :: "s"(ldsw), "v"(v0), "s"(gbase), "i"(LOFF), "i"(OFF) : "memory", "scc");
}
template <int OFF> DI void dma_nt(unsigned v0, const void* gbase) {
  asm volatile("global_load_lds_dwordx4 %0, %1 offset:%2 nt" :: "v"(v0), "s"(gbase), "i"(OFF) : "memory");
}
template <int LOFF> DI void dma_m0(unsigned ldsw, unsigned v0, const void* gbase) {
  asm volatile("s_add_u32 m0, %0, %3\n\ts_nop 0\n\tglobal_load_lds_dwordx4 %1, %2" :: "s"(ldsw), "v"(v0), "s"(gbase), "i"(LOFF) : "memory", "scc");
}
#define TS(v) do { } while (0)
constexpr int TILE = 8192;

__global__ __launch_bounds__(128, 1)
void attn_kernel(const __hip_bfloat16* __restrict__ xA, const __hip_bfloat16* __restrict__ kA, const __hip_bfloat16* __restrict__ vA, const __hip_bfloat16* __restrict__ wqA,
                 const __hip_bfloat16* __restrict__ xB, const __hip_bfloat16* __restrict__ kB, const __hip_bfloat16* __restrict__ vB, const __hip_bfloat16* __restrict__ wqB,
                 __hip_bfloat16* __restrict__ ows) {
  __shared__ __attribute__((aligned(16))) char smem[10 * TILE];
  const unsigned lds0 = (unsigned)(size_t)smem;
  const int tid = threadIdx.x, lane = tid & 63;
  const int w = __builtin_amdgcn_readfirstlane(tid >> 6);
  const int li = lane & 15, g = lane >> 4, q4 = (lane >> 2) & 3, p4 = lane & 3;

  const int idx = blockIdx.x, xcd = idx & 7, j0 = idx >> 3, blk = j0 >> 6, jj = j0 & 63;
  const int b = (xcd >> 2) * 8 + (jj >> 3), h = (xcd & 3) * 8 + (jj & 7);
  const char* xg = (const char*)(blk ? xB : xA) + (size_t)b * SQ * DM * 2;
  const char* wqg = (const char*)(blk ? wqB : wqA) + (size_t)h * DH * 2 + (size_t)w * 16 * 8192;
  const char* kg = (const char*)(blk ? kB : kA) + ((size_t)b * NH + h) * SK * DH * 2 + w * 4096;
  const char* vg = (const char*)(blk ? vB : vA) + ((size_t)b * NH + h) * SK * DH * 2 + w * 4096;
  const unsigned ldsw = lds0 + w * 4096;

  unsigned voffK[4], voffV[2], voffW[2];
#pragma unroll
  for (int n = 0; n < 4; ++n) voffK[n] = g * 256 + (((li ^ g) ^ (4 * n)) * 16);
#pragma unroll
  for (int n = 0; n < 2; ++n) voffV[n] = g * 256 + ((li ^ (g << 1) ^ (n << 3)) * 16);
#pragma unroll
  for (int n = 0; n < 2; ++n) voffW[n] = g * 8192 + ((li ^ ((g | (n << 2)) << 1)) * 16);
  unsigned kofs[4], wofs[4], vofs[8];
#pragma unroll
  for (int ks = 0; ks < 4; ++ks) kofs[ks] = lds0 + li * 256 + (((4 * ks + g) ^ li) * 16);
  const unsigned r7 = 4 * (g & 1) + q4;
#pragma unroll
  for (int db = 0; db < 8; ++db) vofs[db] = lds0 + (4 * g + q4) * 256 + (r7 ^ db) * 32 + (p4 >> 1) * 16 + (p4 & 1) * 8;
#pragma unroll
  for (int t = 0; t < 4; ++t) wofs[t] = lds0 + 6 * TILE + (8 * g + q4) * 256 + (((4 * t + p4) ^ (2 * q4 | ((g & 1) << 3))) * 16);
  unsigned xoff[5];
#pragma unroll
  for (int rb = 0; rb < 5; ++rb) xoff[rb] = (80 * w + 16 * rb + li) * 1024 + g * 16;

  unsigned long long ts0 = 0, ts1 = 0, ts2 = 0, ts3 = 0, ts4 = 0, tw0 = 0, tw1 = 0, twsum = 0, tqw = 0, tq0 = 0;
  (void)ts0; (void)ts1; (void)ts2; (void)ts3; (void)ts4; (void)tw0; (void)tw1; (void)twsum; (void)tqw; (void)tq0;
  TS(ts0);

  auto pieceW = [&](auto N, auto PC) { constexpr int n = decltype(N)::value, pc = decltype(PC)::value;
    dma_m0<(6 + n % 4) * TILE + pc * 1024>(ldsw, voffW[(pc >> 1) & 1], wqg + (size_t)(32 * n) * 8192 + (size_t)pc * 32768); };
  auto pieceK = [&](auto SL, auto PC, const char* gb) { constexpr int sl = decltype(SL)::value, pc = decltype(PC)::value;
    if constexpr (pc == 0) dma_nt_m0<sl * TILE, 0>(ldsw, voffK[0], gb); else dma_nt<pc * 1024>(voffK[pc], gb); };
  auto pieceV = [&](auto SL, auto PC, const char* gb) { constexpr int sl = decltype(SL)::value, pc = decltype(PC)::value;
    if constexpr (pc == 0) dma_nt_m0<sl * TILE, 0>(ldsw, voffV[0], gb); else dma_nt<pc * 1024>(voffV[pc & 1], gb); };

  s16x8 xf[6][5], wf[2][8];
  auto xload = [&](auto N, auto I) { constexpr int n = decltype(N)::value, i = decltype(I)::value;
    xf[(n + (i & 1)) % 6][i >> 1] = gload16<64 * (n + (i & 1))>(xoff[i >> 1], xg); };
  auto wread = [&](auto N, auto I) { constexpr int n = decltype(N)::value, i = decltype(I)::value;
    const unsigned a = wofs[i >> 1] + (n % 4) * TILE + (i & 1) * 8; wf[n & 1][i] = cat8(lds_tr(a), lds_tr(a + 1024)); };
  sfor<10>([&](auto I) { xload(IC<0>{}, I); });
  sfor<4>([&](auto P) { pieceW(IC<0>{}, P); }); sfor<4>([&](auto P) { pieceW(IC<1>{}, P); });
  sfor<10>([&](auto I) { xload(IC<2>{}, I); });
  sfor<4>([&](auto P) { pieceW(IC<2>{}, P); }); sfor<4>([&](auto P) { pieceW(IC<3>{}, P); });
  asm volatile("" ::: ACLOB);
  sfor<40>([&](auto I) { acc_zero4<4 * decltype(I)::value>(); });
  TS(tq0); tq0 -= ts0;
  WAITV(22); BARRIER();
  sfor<8>([&](auto I) { wread(IC<0>{}, I); });
  sfor<4>([&](auto P) { pieceK(IC<0>{}, P, kg); });
  auto qstep = [&](auto N, auto WNC) {
    constexpr int n = decltype(N)::value;
    TS(tw0);
    asm volatile("s_waitcnt vmcnt(%0) lgkmcnt(0)" :: "i"(decltype(WNC)::value) : "memory");
    PIN5(xf[n % 6]);
    BARRIER();
    TS(tw1); tqw += tw1 - tw0;
    SB();
    constexpr bool hasW = n + 4 < 16, hasX = (n & 1) == 0 && n + 4 < 16;
    constexpr int kvt = n == 0 ? 1 : n == 1 ? 2 : n == 2 ? 3 : n == 3 ? 4 : n == 4 ? 5 : n == 13 ? 6 : -1;
    constexpr int nW = hasW ? 4 : 0, nX = hasX ? 10 : 0, nKV = kvt >= 0 ? 4 : 0;
    sfor<40>([&](auto M) {
      constexpr int m = decltype(M)::value, d = m / 5, rb = m % 5;
      mfma_acc<AO(rb, d)>(wf[n & 1][d], xf[n % 6][rb]);
      if constexpr (n < 15 && m >= 1 && m <= 16 && (m & 1)) wread(IC<n + 1>{}, IC<(m - 1) / 2>{});
      if constexpr ((m & 1) == 0) {
        constexpr int it = m / 2;
        if constexpr (it < nW) pieceW(IC<hasW ? n + 4 : 0>{}, IC<it>{});
        else if constexpr (it < nW + nX) xload(IC<hasX ? n + 4 : 0>{}, IC<it - nW>{});
        else if constexpr (it < nW + nX + nKV) {
          constexpr int s_ = kvt >= 0 ? kvt : 0;
          if constexpr ((s_ & 1) == 0) pieceK(IC<s_>{}, IC<it - nW - nX>{}, kg + (size_t)(s_ / 2) * 8192);
          else pieceV(IC<s_>{}, IC<it - nW - nX>{}, vg + (size_t)(s_ / 2) * 8192);
        }
      }
      SB();
    });
  };
  qstep(IC<0>{}, IC<22>{}); qstep(IC<1>{}, IC<26>{}); qstep(IC<2>{}, IC<30>{}); qstep(IC<3>{}, IC<40>{});
  qstep(IC<4>{}, IC<30>{}); qstep(IC<5>{}, IC<40>{}); qstep(IC<6>{}, IC<26>{}); qstep(IC<7>{}, IC<32>{});
  qstep(IC<8>{}, IC<18>{}); qstep(IC<9>{}, IC<28>{}); qstep(IC<10>{}, IC<18>{}); qstep(IC<11>{}, IC<28>{});
  qstep(IC<12>{}, IC<18>{}); qstep(IC<13>{}, IC<14>{}); qstep(IC<14>{}, IC<4>{}); qstep(IC<15>{}, IC<4>{});
  asm volatile("s_nop 7\n\ts_nop 7" ::: "memory");
  sfor<5>([&](auto R) { sfor<4>([&](auto Tt) {
    constexpr int rb = decltype(R)::value, t = decltype(Tt)::value;
    float v[8];
    sfor<8>([&](auto E) { v[decltype(E)::value] = acc_rd<AO(rb, 2 * t) + decltype(E)::value>() * QSCALE; });
    sfor<4>([&](auto E) { constexpr int e = decltype(E)::value; acc_wr<AQ(rb, t) + e>(cvtpk(v[2 * e], v[2 * e + 1])); });
  }); });
  sfor<40>([&](auto I) { acc_zero4<4 * decltype(I)::value>(); });

  f32x4 L[5], NM[5], SA[5][2], SBB[5][2];
  unsigned Pw[5][4];
  s16x8 vf[8], kf[2][4];
  s16x8 ONES = {0x3F80, 0x3F80, 0x3F80, 0x3F80, 0x3F80, 0x3F80, 0x3F80, 0x3F80};
  asm volatile("" : "+v"(ONES));
#pragma unroll
  for (int rb = 0; rb < 5; ++rb) { L[rb] = f32x4{0.f, 0.f, 0.f, 0.f}; NM[rb] = f32x4{0.f, 0.f, 0.f, 0.f}; Pw[rb][0] = Pw[rb][1] = Pw[rb][2] = Pw[rb][3] = 0u; }
#pragma unroll
  for (int db = 0; db < 8; ++db) vf[db] = s16x8{0, 0, 0, 0, 0, 0, 0, 0};

  auto rowmax = [&](const f32x4 (&Sr)[2]) {
    float t = max3f(Sr[0][0], Sr[0][1], Sr[0][2]);
    t = max3f(t, Sr[0][3], Sr[1][0]); t = max3f(t, Sr[1][1], Sr[1][2]); t = max2f(t, Sr[1][3]);
    return t;
  };
  auto xlane_max = [&](float t) { t = fmaxf(t, __shfl_xor(t, 16)); t = fmaxf(t, __shfl_xor(t, 32)); return t; };
  auto sm_item = [&](f32x4 (&S)[5][2], int rb, int k) {
    if (k < 8) { float t = __builtin_amdgcn_exp2f(S[rb][k >> 2][k & 3]); asm volatile("" : "+v"(t)); S[rb][k >> 2][k & 3] = t; }
    else { const int c = k - 8; unsigned t = cvtpk(S[rb][c >> 1][(c & 1) * 2], S[rb][c >> 1][(c & 1) * 2 + 1]); asm volatile("" : "+v"(t)); Pw[rb][c] = t; }
  };
  auto Pf = [&](int rb) { u32x4 v = {Pw[rb][0], Pw[rb][1], Pw[rb][2], Pw[rb][3]}; return __builtin_bit_cast(s16x8, v); };
  auto rescale = [&](f32x4 (&S)[5][2], auto R0C, auto R1C) {
    constexpr int R0 = decltype(R0C)::value, R1 = decltype(R1C)::value;
    asm volatile("s_nop 7\n\ts_nop 7" : "+v"(L[0]), "+v"(L[1]), "+v"(L[2]), "+v"(L[3]), "+v"(L[4]));
    sfor<R1 - R0>([&](auto R) {
      constexpr int rb = R0 + decltype(R)::value;
      const float dl = fmaxf(xlane_max(rowmax(S[rb])), 0.f);
      const float al = __builtin_amdgcn_exp2f(-dl);
      S[rb][0] -= dl; S[rb][1] -= dl; NM[rb] -= dl; L[rb] *= al;
      sfor<32>([&](auto E) { acc_scale<AO(rb, 0) + decltype(E)::value>(al); });
    });
    asm volatile("s_nop 1" : "+v"(L[0]), "+v"(L[1]), "+v"(L[2]), "+v"(L[3]), "+v"(L[4]), "+v"(NM[0]), "+v"(NM[1]), "+v"(NM[2]), "+v"(NM[3]), "+v"(NM[4]));
  };

  TS(ts1);
  {
    WAITV(4); BARRIER();
#pragma unroll
    for (int kb = 0; kb < 2; ++kb)
#pragma unroll
      for (int ks = 0; ks < 4; ++ks) kf[kb][ks] = lds_b128(kofs[ks] + kb * 4096);
    asm volatile("s_nop 1" : "+v"(NM[0]), "+v"(NM[1]), "+v"(NM[2]), "+v"(NM[3]), "+v"(NM[4]) :: "memory");
    sfor<8>([&](auto G8) { sfor<5>([&](auto R) {
      constexpr int kb = decltype(G8)::value >> 2, ks = decltype(G8)::value & 3, rb = decltype(R)::value;
      if constexpr (ks == 0) mfma_qk0<AQ(rb, ks)>(SA[rb][kb], kf[kb][ks], NM[rb]); else mfma_qk<AQ(rb, ks)>(SA[rb][kb], kf[kb][ks]);
    }); });
    asm volatile("s_nop 7\n\ts_nop 7" : "+v"(SA[0][0]), "+v"(SA[0][1]), "+v"(SA[1][0]), "+v"(SA[1][1]), "+v"(SA[2][0]), "+v"(SA[2][1]), "+v"(SA[3][0]), "+v"(SA[3][1]), "+v"(SA[4][0]), "+v"(SA[4][1]));
#pragma unroll
    for (int rb = 0; rb < 5; ++rb) {
      const float m = xlane_max(rowmax(SA[rb]));
      SA[rb][0] -= m; SA[rb][1] -= m; NM[rb] -= m;
    }
  }
  TS(ts2);
  auto aiter = [&](int j, f32x4 (&SC)[5][2], f32x4 (&SN)[5][2], auto PHC, auto MODEC, auto WNC) {
    constexpr int PH = decltype(PHC)::value, MODE = decltype(MODEC)::value, WN = decltype(WNC)::value;
    constexpr bool LAST = (MODE & 4) != 0;
    constexpr bool doK = (MODE & 1) != 0, doV = (MODE & 2) != 0;
    constexpr int KS = 2 * ((PH + 1) % 4), VS = 2 * PH + 1;
    constexpr int KD = 2 * PH, VD = 2 * ((PH + 3) % 4) + 1;
    const char* kgn = kg + (size_t)(j + 4) * 8192; const char* vgn = vg + (size_t)(j + 3) * 8192;
    TS(tw0);
    asm volatile("s_waitcnt vmcnt(%0)" :: "i"(WN) : "memory");
    BARRIER();
    TS(tw1); twsum += tw1 - tw0;
    SB();
    float tm2 = 0.f;
    sfor<18>([&](auto M) {
      constexpr int m = decltype(M)::value, rb = 3 + m / 9, db = m % 9;
      if constexpr (!LAST && m < 8) kf[m >> 2][m & 3] = lds_b128(kofs[m & 3] + KS * TILE + (m >> 2) * 4096);
      if constexpr (db < 8) mfma_acc<AO(rb, db)>(vf[db], Pf(rb)); else mfma_v(L[rb], ONES, Pf(rb));
      if constexpr (m < 8) {
        constexpr int r2 = 3 + (m >> 2), kb = (m >> 1) & 1, hf = m & 1;
        if constexpr (m == 0) tm2 = max2f(SC[3][0][0], SC[3][0][1]); else tm2 = max3f(tm2, SC[r2][kb][2 * hf], SC[r2][kb][2 * hf + 1]);
      } else if constexpr (m < 16) sm_item(SC, 0, m - 8);
      else { sm_item(SC, 0, 8 + 2 * (m - 16)); sm_item(SC, 0, 9 + 2 * (m - 16)); }
      SB();
    });
    if (__builtin_expect(__any(tm2 > THR), 0)) rescale(SC, IC<3>{}, IC<5>{});
    if constexpr (!LAST) {
      sfor<40>([&](auto M) {
        constexpr int m = decltype(M)::value, g8 = m / 5, rb = m % 5, kb = g8 >> 2, ks = g8 & 3;
        if constexpr (ks == 0) mfma_qk0<AQ(rb, ks)>(SN[rb][kb], kf[kb][ks], NM[rb]); else mfma_qk<AQ(rb, ks)>(SN[rb][kb], kf[kb][ks]);
        constexpr int r5 = m % 5;
        if constexpr (r5 == 0) {
          constexpr int pc = m / 5;
          if constexpr (pc < 4) { if constexpr (doK) pieceK(IC<KD>{}, IC<pc>{}, kgn); }
          else { if constexpr (doV) pieceV(IC<VD>{}, IC<pc - 4>{}, vgn); }
        } else if constexpr (r5 == 1) {
          constexpr int db = m / 5; const unsigned a = vofs[db] + VS * TILE; vf[db] = cat8(lds_tr(a), lds_tr(a + 4096));
        } else {
          constexpr int it = (m / 5) * 3 + (r5 - 2);
          sm_item(SC, 1 + it / 12, it % 12);
        }
        SB();
      });
    } else {
#pragma unroll
      for (int db = 0; db < 8; ++db) { const unsigned a = vofs[db] + VS * TILE; vf[db] = cat8(lds_tr(a), lds_tr(a + 4096)); }
#pragma unroll
      for (int k = 0; k < 24; ++k) sm_item(SC, 1 + k / 12, k % 12);
      asm volatile("s_nop 1" ::: "memory");
      SB();
    }
    float tm1 = 0.f, tm1b = 0.f;
    sfor<27>([&](auto M) {
      constexpr int m = decltype(M)::value, rb = m / 9, db = m % 9;
      if constexpr (db < 8) mfma_acc<AO(rb, db)>(vf[db], Pf(rb)); else mfma_v(L[rb], ONES, Pf(rb));
      constexpr int e = m < 8 ? m : (m >= 12 && m < 20) ? 12 + (m - 12) : -1;
      constexpr int c = (m >= 8 && m < 12) ? m : (m >= 20 && m < 24) ? m : -1;
      if constexpr (e >= 0) sm_item(SC, 3 + e / 12, e % 12);
      if constexpr (c >= 0) sm_item(SC, 3 + c / 12, c % 12);
      if constexpr (!LAST) {
        constexpr int x = (m >= 8 && m < 12) ? m - 8 : (m >= 20 && m < 24) ? 4 + (m - 20) : -1;
        auto mstep = [&](auto XC) { constexpr int xx = decltype(XC)::value, kb = xx / 6, r2 = (xx % 6) >> 1, hf = xx & 1;
          if constexpr (xx == 0) tm1 = max2f(SN[0][0][0], SN[0][0][1]); else if constexpr (xx == 1) tm1b = max2f(SN[0][0][2], SN[0][0][3]);
          else if constexpr (hf == 0) tm1 = max3f(tm1, SN[r2][kb][0], SN[r2][kb][1]); else tm1b = max3f(tm1b, SN[r2][kb][2], SN[r2][kb][3]); };
        if constexpr (x >= 0) mstep(IC<x>{});
        if constexpr (m == 24) { mstep(IC<8>{}); mstep(IC<9>{}); }
        if constexpr (m == 25) { mstep(IC<10>{}); mstep(IC<11>{}); }
      }
      SB();
    });
    if constexpr (!LAST) { if (__builtin_expect(__any(max2f(tm1, tm1b) > THR), 0)) rescale(SN, IC<0>{}, IC<3>{}); }
  };
  for (int j = 0; j < 28; j += 4) {
    aiter(j, SA, SBB, IC<0>{}, IC<3>{}, IC<16>{});
    aiter(j + 1, SBB, SA, IC<1>{}, IC<3>{}, IC<16>{});
    aiter(j + 2, SA, SBB, IC<2>{}, IC<3>{}, IC<16>{});
    aiter(j + 3, SBB, SA, IC<3>{}, IC<3>{}, IC<16>{});
  }
  aiter(28, SA, SBB, IC<0>{}, IC<2>{}, IC<16>{});
  aiter(29, SBB, SA, IC<1>{}, IC<0>{}, IC<12>{});
  aiter(30, SA, SBB, IC<2>{}, IC<0>{}, IC<4>{});
  aiter(31, SBB, SA, IC<3>{}, IC<4>{}, IC<0>{});
  asm volatile("s_nop 1" ::: "memory");
  sfor<18>([&](auto M) {
    constexpr int m = decltype(M)::value, rb = 3 + m / 9, db = m % 9;
    if constexpr (db < 8) mfma_acc<AO(rb, db)>(vf[db], Pf(rb)); else mfma_v(L[rb], ONES, Pf(rb));
  });

  TS(ts3);
  asm volatile("s_nop 7\n\ts_nop 7" : "+v"(L[0]), "+v"(L[1]), "+v"(L[2]), "+v"(L[3]), "+v"(L[4]));
  char* ob = (char*)ows + ((((size_t)(blk * NB + b) * 10 + 5 * w) * NH + h) * 4) * 1024 + lane * 16;
  sfor<5>([&](auto R) {
    constexpr int rb = decltype(R)::value;
    const float il = __builtin_amdgcn_rcpf(L[rb][0]);
    sfor<4>([&](auto Tt) {
      constexpr int t = decltype(Tt)::value;
      float v[8];
      sfor<8>([&](auto E) { v[decltype(E)::value] = acc_rd<AO(rb, 2 * t) + decltype(E)::value>() * il; });
      u32x4 o = {cvtpk(v[0], v[1]), cvtpk(v[2], v[3]), cvtpk(v[4], v[5]), cvtpk(v[6], v[7])};
      *(u32x4*)(ob + (size_t)rb * (NH * 4 * 1024) + t * 1024) = o;
    });
  });
}

constexpr int K2_NBUF = 4;
constexpr int K2_LDS = 3 * 4 * 10240 * 1;
__global__ __launch_bounds__(256, 1)
void oproj_kernel(const __hip_bfloat16* __restrict__ ows, const __hip_bfloat16* __restrict__ woA, const __hip_bfloat16* __restrict__ woB,
                  __hip_bfloat16* __restrict__ out) {
  __shared__ __attribute__((aligned(16))) char smem[K2_LDS];
  const unsigned lds0 = (unsigned)(size_t)smem;
  const int tid = threadIdx.x, lane = tid & 63;
  const int w = __builtin_amdgcn_readfirstlane(tid >> 6);
  const int li = lane & 15, g = lane >> 4, q4 = (lane >> 2) & 3, p4 = lane & 3;
  const int idx = blockIdx.x, xcd = idx & 7, r = idx >> 3;
  const int nt = r & 7, bb = (r >> 3) * 8 + xcd;
  const int blk = bb >> 4;
  const char* og = (const char*)ows + (size_t)bb * 10 * 128 * 1024 + (size_t)w * 32 * 1024;
  const char* wog = (const char*)(blk ? woB : woA) + (size_t)(w * 1024) * 1024 + nt * 128;
  const unsigned rowl = lane >> 3, pos = lane & 7;
  const unsigned voffw = rowl * 1024 + (((((pos >> 1) ^ ((rowl >> 1) & 3)) << 1) | (pos & 1)) * 16);
  const unsigned wbase = lds0 + w * (K2_NBUF * 4096);
  const unsigned rr = 4 * g + q4;
  const unsigned trb = wbase + rr * 128 + ((rr >> 1) & 3) * 32 + p4 * 8;
  const unsigned ovoff = lane * 16;

  auto issueWo = [&](int sl, int ks) {
    const unsigned lb = wbase + (sl & (K2_NBUF - 1)) * 4096;
    const char* gb = wog + (size_t)ks * 32 * 1024;
#pragma unroll
    for (int pc = 0; pc < 4; ++pc) dma1(lb + pc * 1024, voffw, gb + pc * 8192);
  };
  f32x4 acc[10][4];
#pragma unroll
  for (int rb = 0; rb < 10; ++rb)
#pragma unroll
    for (int nb = 0; nb < 4; ++nb) acc[rb][nb] = f32x4{0.f, 0.f, 0.f, 0.f};
  s16x8 of[2][10];
  auto loadO = [&](s16x8 (&dst)[10], int ks) {
#pragma unroll
    for (int rb = 0; rb < 10; ++rb) dst[rb] = gload16<0>(ovoff, og + (size_t)rb * 131072 + (size_t)ks * 1024);
  };
#define PIN10(a) asm volatile("" : "+v"(a[0]), "+v"(a[1]), "+v"(a[2]), "+v"(a[3]), "+v"(a[4]), "+v"(a[5]), "+v"(a[6]), "+v"(a[7]), "+v"(a[8]), "+v"(a[9]))
  auto compute = [&](s16x8 (&src)[10], int ks) {
    const unsigned a0 = trb + (ks & (K2_NBUF - 1)) * 4096;
#pragma unroll
    for (int nb = 0; nb < 4; ++nb) {
      const unsigned a = a0 ^ (nb << 5);
      const s16x8 wf = cat8(lds_tr(a), lds_tr(a + 2048));
#pragma unroll
      for (int rb = 0; rb < 10; ++rb) acc[rb][nb] = mfma16(wf, src[rb], acc[rb][nb]);
    }
  };
  issueWo(0, 0); issueWo(1, 1); loadO(of[0], 0);
  for (int ks = 0; ks < 32; ks += 2) {
    issueWo(ks + 2, ks + 2 < 32 ? ks + 2 : 31);
    loadO(of[1], ks + 1);
    WAITV(14); PIN10(of[0]);
    compute(of[0], ks);
    issueWo(ks + 3, ks + 3 < 32 ? ks + 3 : 31);
    loadO(of[0], ks + 2 < 32 ? ks + 2 : 31);
    WAITV(14); PIN10(of[1]);
    compute(of[1], ks + 1);
  }
  WAITV(0);
  __syncthreads();
#pragma unroll
  for (int nb = 0; nb < 4; ++nb) {
    if (nb != w) {
      const int sl = (w - nb - 1) & 3;
#pragma unroll
      for (int rb = 0; rb < 10; ++rb) *(lds_f32x4_ptr)(lds0 + ((nb * 3 + sl) * 10 + rb) * 1024 + lane * 16) = acc[rb][nb];
    }
  }
  __syncthreads();
  char* outp = (char*)out + (size_t)(bb & 15) * SQ * 2048 + blk * 1024 + nt * 128 + w * 32 + g * 8;
#pragma unroll
  for (int rb = 0; rb < 10; ++rb) {
    f32x4 s;
    if (w == 0) s = acc[rb][0]; else if (w == 1) s = acc[rb][1]; else if (w == 2) s = acc[rb][2]; else s = acc[rb][3];
#pragma unroll
    for (int sl = 0; sl < 3; ++sl) s += *(lds_f32x4_ptr)(lds0 + ((w * 3 + sl) * 10 + rb) * 1024 + lane * 16);
    uint2 o2; o2.x = cvtpk(s[0], s[1]); o2.y = cvtpk(s[2], s[3]);
    *(uint2*)(outp + (size_t)(16 * rb + li) * 2048) = o2;
  }
}

extern "C" void kernel_launch(void* const* d_in, const int* in_sizes, int n_in,
                              void* d_out, int out_size, void* d_ws, size_t ws_size,
                              hipStream_t stream) {
  const __hip_bfloat16* xA = (const __hip_bfloat16*)d_in[0];
  const __hip_bfloat16* kA = (const __hip_bfloat16*)d_in[1];
  const __hip_bfloat16* vA = (const __hip_bfloat16*)d_in[2];
  const __hip_bfloat16* wqA = (const __hip_bfloat16*)d_in[3];
  const __hip_bfloat16* woA = (const __hip_bfloat16*)d_in[4];
  const __hip_bfloat16* xB = (const __hip_bfloat16*)d_in[5];
  const __hip_bfloat16* kB = (const __hip_bfloat16*)d_in[6];
  const __hip_bfloat16* vB = (const __hip_bfloat16*)d_in[7];
  const __hip_bfloat16* wqB = (const __hip_bfloat16*)d_in[8];
  const __hip_bfloat16* woB = (const __hip_bfloat16*)d_in[9];
  __hip_bfloat16* ows = (__hip_bfloat16*)d_ws;
  attn_kernel<<<1024, 128, 0, stream>>>(xA, kA, vA, wqA, xB, kB, vB, wqB, ows);
  oproj_kernel<<<256, 256, 0, stream>>>(ows, woA, woB, (__hip_bfloat16*)d_out);
}
```

```cpp
#include <hip/hip_runtime.h>
#include <hip/hip_bf16.h>
#include <utility>
#include <type_traits>

typedef __attribute__((ext_vector_type(8))) short s16x8;
typedef __attribute__((ext_vector_type(4))) short s16x4;
typedef __attribute__((ext_vector_type(4))) float f32x4;
typedef __attribute__((ext_vector_type(2))) float f32x2;
typedef __attribute__((ext_vector_type(2))) __bf16 bf16x2;
typedef __attribute__((ext_vector_type(4))) unsigned u32x4;
typedef __attribute__((address_space(3))) s16x4* lds_s16x4_ptr;
typedef __attribute__((address_space(3))) s16x8* lds_s16x8_ptr;
typedef __attribute__((address_space(3))) f32x4* lds_f32x4_ptr;

#define DI __device__ __forceinline__

constexpr int NB = 16, SQ = 160, DM = 512, NH = 32, SK = 1024, DH = 128;
constexpr int NS = 5;
constexpr int SLOT = 16384;
constexpr float QSCALE = 0.088388347648318440f * 1.4426950408889634f;
constexpr float THR = 8.0f;

DI unsigned cvtpk(float lo, float hi) { f32x2 v = {lo, hi}; bf16x2 b = __builtin_convertvector(v, bf16x2); return __builtin_bit_cast(unsigned, b); }
DI s16x8 pack8(f32x4 a, f32x4 b) { u32x4 w = {cvtpk(a[0], a[1]), cvtpk(a[2], a[3]), cvtpk(b[0], b[1]), cvtpk(b[2], b[3])}; return __builtin_bit_cast(s16x8, w); }
DI float max3f(float a, float b, float c) { float r; asm("v_max3_f32 %0, %1, %2, %3" : "=v"(r) : "v"(a), "v"(b), "v"(c)); return r; }
DI float max2f(float a, float b) { float r; asm("v_max_f32_e32 %0, %1, %2" : "=v"(r) : "v"(a), "v"(b)); return r; }
DI f32x4 mfma16(s16x8 a, s16x8 b, f32x4 c) { return __builtin_amdgcn_mfma_f32_16x16x32_bf16(a, b, c, 0, 0, 0); }
DI s16x8 lds_b128(unsigned addr) { return *(lds_s16x8_ptr)(addr); }
DI s16x4 lds_tr(unsigned addr) { return __builtin_amdgcn_ds_read_tr16_b64_v4i16((lds_s16x4_ptr)(addr)); }
DI s16x8 cat8(s16x4 a, s16x4 b) { return __builtin_shufflevector(a, b, 0, 1, 2, 3, 4, 5, 6, 7); }

DI void dma4(unsigned ldsaddr, unsigned v0, unsigned v1, unsigned v2, unsigned v3, const void* gbase) {
  asm volatile("s_mov_b32 m0, %0\n\ts_nop 0\n\t"
               "global_load_lds_dwordx4 %1, %5 nt\n\t"
               "global_load_lds_dwordx4 %2, %5 offset:1024 nt\n\t"
               "global_load_lds_dwordx4 %3, %5 offset:2048 nt\n\t"
               "global_load_lds_dwordx4 %4, %5 offset:3072 nt"
               :: "s"(ldsaddr), "v"(v0), "v"(v1), "v"(v2), "v"(v3), "s"(gbase) : "memory");
}
DI void dma1(unsigned ldsaddr, unsigned v0, const void* gbase) {
  asm volatile("s_mov_b32 m0, %0\n\ts_nop 0\n\t"
               "global_load_lds_dwordx4 %1, %2"
               :: "s"(ldsaddr), "v"(v0), "s"(gbase) : "memory");
}
template <int OFF> DI s16x8 gload16(unsigned voff, const void* gbase) {
  s16x8 r; asm volatile("global_load_dwordx4 %0, %1, %2 offset:%3" : "=v"(r) : "v"(voff), "s"(gbase), "i"(OFF) : "memory"); return r;
}
#define WAITV(n) asm volatile("s_waitcnt vmcnt(" #n ")" ::: "memory")
#define BARRIER() asm volatile("s_barrier" ::: "memory")
#define PIN5(a) asm volatile("" : "+v"(a[0]), "+v"(a[1]), "+v"(a[2]), "+v"(a[3]), "+v"(a[4]))

#define ACLOB "a0","a1","a2","a3","a4","a5","a6","a7","a8","a9","a10","a11","a12","a13","a14","a15","a16","a17","a18","a19","a20","a21","a22","a23","a24","a25","a26","a27","a28","a29","a30","a31","a32","a33","a34","a35","a36","a37","a38","a39","a40","a41","a42","a43","a44","a45","a46","a47","a48","a49","a50","a51","a52","a53","a54","a55","a56","a57","a58","a59","a60","a61","a62","a63","a64","a65","a66","a67","a68","a69","a70","a71","a72","a73","a74","a75","a76","a77","a78","a79","a80","a81","a82","a83","a84","a85","a86","a87","a88","a89","a90","a91","a92","a93","a94","a95","a96","a97","a98","a99","a100","a101","a102","a103","a104","a105","a106","a107","a108","a109","a110","a111","a112","a113","a114","a115","a116","a117","a118","a119","a120","a121","a122","a123","a124","a125","a126","a127"
#define SB() __builtin_amdgcn_sched_barrier(0)
template <int... I, class F> DI void sfor_impl(std::integer_sequence<int, I...>, F&& f) { (f(std::integral_constant<int, I>{}), ...); }
template <int N, class F> DI void sfor(F&& f) { sfor_impl(std::make_integer_sequence<int, N>{}, f); }
template <int N> using IC = std::integral_constant<int, N>;
constexpr int AO(int rb, int db) { return (rb * 8 + db) * 4; }
constexpr int AQ(int f) { return 96 + f * 4; }
template <int A> DI void mfma_acc(s16x8 a, s16x8 b) {
  asm volatile("v_mfma_f32_16x16x32_bf16 a[%c2:%c3], %0, %1, a[%c2:%c3]" :: "v"(a), "v"(b), "i"(A), "i"(A + 3));
}
template <int A> DI void mfma_qk0(f32x4& S, s16x8 kf, const f32x4& nm) {
  asm volatile("v_mfma_f32_16x16x32_bf16 %0, %1, a[%c3:%c4], %2" : "=&v"(S) : "v"(kf), "v"(nm), "i"(A), "i"(A + 3));
}
template <int A> DI void mfma_qk(f32x4& S, s16x8 kf) {
  asm volatile("v_mfma_f32_16x16x32_bf16 %0, %1, a[%c2:%c3], %0" : "+v"(S) : "v"(kf), "i"(A), "i"(A + 3));
}
DI void mfma_v0(f32x4& D, s16x8 a, s16x8 b, const f32x4& c) { asm volatile("v_mfma_f32_16x16x32_bf16 %0, %1, %2, %3" : "=&v"(D) : "v"(a), "v"(b), "v"(c)); }
DI void mfma_v(f32x4& D, s16x8 a, s16x8 b) { asm volatile("v_mfma_f32_16x16x32_bf16 %0, %1, %2, %0" : "+v"(D) : "v"(a), "v"(b)); }
template <int A> DI float acc_rd() { float r; asm volatile("v_accvgpr_read_b32 %0, a[%c1]" : "=v"(r) : "i"(A)); return r; }
template <int A> DI void acc_wr(unsigned v) { asm volatile("v_accvgpr_write_b32 a[%c1], %0" :: "v"(v), "i"(A)); }
template <int A> DI void acc_zero4() { asm volatile("v_accvgpr_write_b32 a[%c0], 0\n\tv_accvgpr_write_b32 a[%c1], 0\n\tv_accvgpr_write_b32 a[%c2], 0\n\tv_accvgpr_write_b32 a[%c3], 0" :: "i"(A), "i"(A + 1), "i"(A + 2), "i"(A + 3)); }
template <int A> DI void acc_scale(float f) { float t; asm volatile("v_accvgpr_read_b32 %0, a[%c2]\n\tv_mul_f32 %0, %0, %1\n\tv_accvgpr_write_b32 a[%c2], %0" : "=&v"(t) : "v"(f), "i"(A)); }
DI float fadd_s(float a, float b) { float r; asm("v_add_f32_e32 %0, %1, %2" : "=v"(r) : "v"(a), "v"(b)); return r; }
template <int LOFF> DI void dma4_nt(unsigned ldsw, unsigned v0, unsigned v1, unsigned v2, unsigned v3, const void* gbase) {
  asm volatile("s_add_u32 m0, %0, %6\n\ts_nop 0\n\t"
               "global_load_lds_dwordx4 %1, %5 nt\n\t"
               "global_load_lds_dwordx4 %2, %5 offset:1024 nt\n\t"
               "global_load_lds_dwordx4 %3, %5 offset:2048 nt\n\t"
               "global_load_lds_dwordx4 %4, %5 offset:3072 nt"
               :: "s"(ldsw), "v"(v0), "v"(v1), "v"(v2), "v"(v3), "s"(gbase), "i"(LOFF) : "memory", "scc");
}
template <int LOFF> DI void dma_m0(unsigned ldsw, unsigned v0, const void* gbase) {
  asm volatile("s_add_u32 m0, %0, %3\n\ts_nop 0\n\tglobal_load_lds_dwordx4 %1, %2" :: "s"(ldsw), "v"(v0), "s"(gbase), "i"(LOFF) : "memory", "scc");
}
#define TS(v) do { } while (0)
constexpr int TILE = 8192;

template <int NRB>
DI void attn_body(const char* xg, const char* wqg, const char* kg, const char* vg, char* ob, const unsigned ldsh, const int r, const int row0, const int lane
                  ) {
  constexpr bool SMALL = NRB == 2;
  const int li = lane & 15, g = lane >> 4, q4 = (lane >> 2) & 3, p4 = lane & 3;
  const unsigned ldsw = ldsh + r * 4096;
  unsigned voffK[4], voffV[2], voffW[2];
#pragma unroll
  for (int n = 0; n < 4; ++n) voffK[n] = g * 256 + (((li ^ g) ^ (4 * n)) * 16);
#pragma unroll
  for (int n = 0; n < 2; ++n) voffV[n] = g * 256 + ((li ^ (g << 1) ^ (n << 3)) * 16);
#pragma unroll
  for (int n = 0; n < 2; ++n) voffW[n] = g * 8192 + ((li ^ ((g | (n << 2)) << 1)) * 16);
  unsigned kofs[4], wofs[4], vofs[8];
#pragma unroll
  for (int ks = 0; ks < 4; ++ks) kofs[ks] = ldsh + li * 256 + (((4 * ks + g) ^ li) * 16);
  const unsigned r7 = 4 * (g & 1) + q4;
#pragma unroll
  for (int db = 0; db < 8; ++db) vofs[db] = ldsh + (4 * g + q4) * 256 + (r7 ^ db) * 32 + (p4 >> 1) * 16 + (p4 & 1) * 8;
#pragma unroll
  for (int t = 0; t < 4; ++t) wofs[t] = ldsh + 6 * TILE + (8 * g + q4) * 256 + (((4 * t + p4) ^ (2 * q4 | ((g & 1) << 3))) * 16);
  unsigned xoff[NRB];
#pragma unroll
  for (int rb = 0; rb < NRB; ++rb) xoff[rb] = (row0 + 16 * rb + li) * 1024 + g * 16;

  unsigned long long ts0 = 0, ts1 = 0, ts3 = 0, ts4 = 0, tw0 = 0, tw1 = 0, twsum = 0, tqw = 0;
  (void)ts0; (void)ts1; (void)ts3; (void)ts4; (void)tw0; (void)tw1; (void)twsum; (void)tqw;
  TS(ts0);

  auto tileW = [&](auto N) { constexpr int n = decltype(N)::value;
    sfor<4>([&](auto PC) { constexpr int pc = decltype(PC)::value;
      dma_m0<(6 + n % 4) * TILE + pc * 1024>(ldsw, voffW[(pc >> 1) & 1], wqg + (size_t)(32 * n) * 8192 + (size_t)pc * 32768); }); };
  auto tileK = [&](auto SL, const char* gb) { dma4_nt<decltype(SL)::value * TILE>(ldsw, voffK[0], voffK[1], voffK[2], voffK[3], gb); };
  auto tileV = [&](auto SL, const char* gb) { dma4_nt<decltype(SL)::value * TILE>(ldsw, voffV[0], voffV[1], voffV[0], voffV[1], gb); };

  constexpr int XA = SMALL ? 4 : 2, XB = XA + 2;
  s16x8 xf[XB][NRB];
  auto xload2 = [&](auto N) { constexpr int n = decltype(N)::value;
    sfor<NRB>([&](auto R) { constexpr int rb = decltype(R)::value;
      xf[n % XB][rb] = gload16<64 * n>(xoff[rb], xg); xf[(n + 1) % XB][rb] = gload16<64 * n + 64>(xoff[rb], xg); }); };
  xload2(IC<0>{});
  if constexpr (SMALL) { tileW(IC<0>{}); tileW(IC<1>{}); xload2(IC<2>{}); tileW(IC<2>{}); }
  asm volatile("" ::: ACLOB);
  sfor<NRB * 8>([&](auto I) { acc_zero4<4 * decltype(I)::value>(); });
  auto qstep = [&](auto N, auto WSC) {
    constexpr int n = decltype(N)::value;
    constexpr int WN = SMALL ? decltype(WSC)::value : ((n & 1) ? 63 : 0);
    TS(tw0);
    asm volatile("s_waitcnt vmcnt(%0)" :: "i"(WN) : "memory");
    if constexpr (NRB == 3) asm volatile("" : "+v"(xf[n % XB][0]), "+v"(xf[n % XB][1]), "+v"(xf[n % XB][2])); else asm volatile("" : "+v"(xf[n % XB][0]), "+v"(xf[n % XB][1]));
    BARRIER();
    TS(tw1); tqw += tw1 - tw0;
    s16x8 wf[8];
#pragma unroll
    for (int i = 0; i < 8; ++i) { const unsigned a = wofs[i >> 1] + (n % 4) * TILE + (i & 1) * 8; wf[i] = cat8(lds_tr(a), lds_tr(a + 1024)); }
    SB();
    if constexpr (SMALL && n + 3 < 16) tileW(IC<n + 3>{});
    if constexpr ((n & 1) == 0 && n + XA < 16) xload2(IC<n + XA>{});
    if constexpr (SMALL && n < 6) { if constexpr ((n & 1) == 0) tileK(IC<n>{}, kg + (size_t)(n / 2) * 8192); else tileV(IC<n>{}, vg + (size_t)(n / 2) * 8192); }
    SB();
    sfor<8 * NRB>([&](auto M) { constexpr int m = decltype(M)::value, d = m / NRB, rb = m % NRB; mfma_acc<AO(rb, d)>(wf[d], xf[n % XB][rb]); });
  };
  qstep(IC<0>{}, IC<12>{}); qstep(IC<1>{}, IC<20>{}); qstep(IC<2>{}, IC<20>{}); qstep(IC<3>{}, IC<28>{});
  qstep(IC<4>{}, IC<24>{}); qstep(IC<5>{}, IC<28>{}); qstep(IC<6>{}, IC<24>{}); qstep(IC<7>{}, IC<24>{});
  qstep(IC<8>{}, IC<16>{}); qstep(IC<9>{}, IC<16>{}); qstep(IC<10>{}, IC<12>{}); qstep(IC<11>{}, IC<16>{});
  qstep(IC<12>{}, IC<12>{}); qstep(IC<13>{}, IC<12>{}); qstep(IC<14>{}, IC<4>{}); qstep(IC<15>{}, IC<0>{});
  asm volatile("s_nop 7\n\ts_nop 7" ::: "memory");
  s16x8 Qv[4];
  sfor<NRB>([&](auto R) { sfor<4>([&](auto Tt) {
    constexpr int rb = decltype(R)::value, t = decltype(Tt)::value, f = rb * 4 + t;
    float v[8];
    sfor<8>([&](auto E) { v[decltype(E)::value] = acc_rd<AO(rb, 2 * t) + decltype(E)::value>() * QSCALE; });
    if constexpr (f < 8) sfor<4>([&](auto E) { constexpr int e = decltype(E)::value; acc_wr<AQ(f) + e>(cvtpk(v[2 * e], v[2 * e + 1])); });
    else { u32x4 q = {cvtpk(v[0], v[1]), cvtpk(v[2], v[3]), cvtpk(v[4], v[5]), cvtpk(v[6], v[7])}; Qv[f - 8] = __builtin_bit_cast(s16x8, q); }
  }); });
  sfor<NRB * 8>([&](auto I) { acc_zero4<4 * decltype(I)::value>(); });

  f32x4 NM[NRB], S[NRB][2];
  float lsum[NRB];
  unsigned Pw[NRB][4];
#pragma unroll
  for (int rb = 0; rb < NRB; ++rb) { lsum[rb] = 0.f; NM[rb] = f32x4{0.f, 0.f, 0.f, 0.f}; }
  auto rowmax = [&](const f32x4 (&Sr)[2]) {
    float t = max3f(Sr[0][0], Sr[0][1], Sr[0][2]);
    t = max3f(t, Sr[0][3], Sr[1][0]); t = max3f(t, Sr[1][1], Sr[1][2]); t = max2f(t, Sr[1][3]);
    return t;
  };
  auto xlane_max = [&](float t) { t = fmaxf(t, __shfl_xor(t, 16)); t = fmaxf(t, __shfl_xor(t, 32)); return t; };
  auto sm_item = [&](int rb, int k) {
    if (k < 8) { float t = __builtin_amdgcn_exp2f(S[rb][k >> 2][k & 3]); asm volatile("" : "+v"(t)); S[rb][k >> 2][k & 3] = t; }
    else if (k < 16) { float t = fadd_s(lsum[rb], S[rb][(k - 8) >> 2][(k - 8) & 3]); asm volatile("" : "+v"(t)); lsum[rb] = t; }
    else { const int c = k - 16; unsigned t = cvtpk(S[rb][c >> 1][(c & 1) * 2], S[rb][c >> 1][(c & 1) * 2 + 1]); asm volatile("" : "+v"(t)); Pw[rb][c] = t; }
  };
  auto Pf = [&](int rb) { u32x4 v = {Pw[rb][0], Pw[rb][1], Pw[rb][2], Pw[rb][3]}; return __builtin_bit_cast(s16x8, v); };

  TS(ts1);
  auto aiter = [&](int j, auto PHC) {
    constexpr int PH = decltype(PHC)::value;
    constexpr int KS = 2 * PH, VS = 2 * PH + 1, KD = 2 * ((PH + 3) % 4), VD = KD + 1;
    TS(tw0);
    if constexpr (SMALL) { if (j < 30) WAITV(16); else WAITV(0); }
    BARRIER();
    TS(tw1); twsum += tw1 - tw0;
    s16x8 kf[2][4], vf[8];
#pragma unroll
    for (int m = 0; m < 8; ++m) kf[m >> 2][m & 3] = lds_b128(kofs[m & 3] + KS * TILE + (m >> 2) * 4096);
    SB();
    if constexpr (SMALL) { if (j + 3 < 32) { tileK(IC<KD>{}, kg + (size_t)(j + 3) * 8192); tileV(IC<VD>{}, vg + (size_t)(j + 3) * 8192); } }
    SB();
    sfor<8 * NRB>([&](auto M) {
      constexpr int m = decltype(M)::value, g8 = m / NRB, rb = m % NRB, kb = g8 >> 2, ks = g8 & 3, f = rb * 4 + ks;
      if constexpr (f < 8) { if constexpr (ks == 0) mfma_qk0<AQ(f)>(S[rb][kb], kf[kb][ks], NM[rb]); else mfma_qk<AQ(f)>(S[rb][kb], kf[kb][ks]); }
      else { if constexpr (ks == 0) mfma_v0(S[rb][kb], kf[kb][ks], Qv[f - 8], NM[rb]); else mfma_v(S[rb][kb], kf[kb][ks], Qv[f - 8]); }
      if constexpr (rb == NRB - 1) { const unsigned a = vofs[g8] + VS * TILE; vf[g8] = cat8(lds_tr(a), lds_tr(a + 4096)); }
      SB();
    });
    if constexpr (NRB == 3) asm volatile("s_nop 7" : "+v"(S[0][0]), "+v"(S[0][1]), "+v"(S[1][0]), "+v"(S[1][1]), "+v"(S[2][0]), "+v"(S[2][1]));
    else asm volatile("s_nop 7" : "+v"(S[0][0]), "+v"(S[0][1]), "+v"(S[1][0]), "+v"(S[1][1]));
    {
      float ta = max2f(S[0][0][0], S[0][0][1]), tb = max2f(S[0][0][2], S[0][0][3]);
#pragma unroll
      for (int i = 1; i < 2 * NRB; ++i) { ta = max3f(ta, S[i >> 1][i & 1][0], S[i >> 1][i & 1][1]); tb = max3f(tb, S[i >> 1][i & 1][2], S[i >> 1][i & 1][3]); }
      const bool first = j == 0;
      if (__builtin_expect(first || __any(max2f(ta, tb) > THR), 0)) {
        sfor<NRB>([&](auto R) {
          constexpr int rb = decltype(R)::value;
          const float rm = xlane_max(rowmax(S[rb]));
          const float dl = first ? rm : fmaxf(rm, 0.f);
          const float al = first ? 1.f : __builtin_amdgcn_exp2f(-dl);
          S[rb][0] -= dl; S[rb][1] -= dl; NM[rb] -= dl; lsum[rb] *= al;
          sfor<32>([&](auto E) { acc_scale<AO(rb, 0) + decltype(E)::value>(al); });
        });
      }
    }
#pragma unroll
    for (int k = 0; k < 20; ++k) sm_item(0, k);
    asm volatile("s_nop 1" ::: "memory");
    SB();
    sfor<8 * NRB>([&](auto M) {
      constexpr int m = decltype(M)::value, rb = m / 8, db = m % 8;
      mfma_acc<AO(rb, db)>(vf[db], Pf(rb));
      if constexpr (rb + 1 < NRB) {
        constexpr int k0 = db < 4 ? 2 * db : 8 + 3 * (db - 4), k1 = db < 4 ? 2 * db + 2 : 8 + 3 * (db - 3);
#pragma unroll
        for (int k = k0; k < k1; ++k) sm_item(rb + 1, k);
        if constexpr (db == 7) asm volatile("s_nop 1" ::: "memory");
      }
      SB();
    });
  };
  for (int j = 0; j < 32; j += 4) { aiter(j, IC<0>{}); aiter(j + 1, IC<1>{}); aiter(j + 2, IC<2>{}); aiter(j + 3, IC<3>{}); }

  TS(ts3);
  asm volatile("s_nop 7\n\ts_nop 7" ::: "memory");
  sfor<NRB>([&](auto R) {
    constexpr int rb = decltype(R)::value;
    float l = lsum[rb]; l += __shfl_xor(l, 16); l += __shfl_xor(l, 32);
    const float il = __builtin_amdgcn_rcpf(l);
    sfor<4>([&](auto Tt) {
      constexpr int t = decltype(Tt)::value;
      float v[8];
      sfor<8>([&](auto E) { v[decltype(E)::value] = acc_rd<AO(rb, 2 * t) + decltype(E)::value>() * il; });
      u32x4 o = {cvtpk(v[0], v[1]), cvtpk(v[2], v[3]), cvtpk(v[4], v[5]), cvtpk(v[6], v[7])};
      *(u32x4*)(ob + (size_t)rb * (NH * 4 * 1024) + t * 1024) = o;
    });
  });
}

__global__ __launch_bounds__(512, 2)
void attn_kernel(const __hip_bfloat16* __restrict__ xA, const __hip_bfloat16* __restrict__ kA, const __hip_bfloat16* __restrict__ vA, const __hip_bfloat16* __restrict__ wqA,
                 const __hip_bfloat16* __restrict__ xB, const __hip_bfloat16* __restrict__ kB, const __hip_bfloat16* __restrict__ vB, const __hip_bfloat16* __restrict__ wqB,
                 __hip_bfloat16* __restrict__ ows) {
  __shared__ __attribute__((aligned(16))) char smem[20 * TILE];
  const unsigned lds0 = (unsigned)(size_t)smem;
  const int tid = threadIdx.x, lane = tid & 63;
  const int w = __builtin_amdgcn_readfirstlane(tid >> 6);
  const int e = w & 1, r = (w >> 1) & 1;
  const int idx = blockIdx.x, xcd = idx & 7, j0 = idx >> 3, blk = j0 >> 5, jj = j0 & 31;
  const int b = (xcd >> 2) * 8 + (jj >> 2), h = ((xcd & 3) * 4 + (jj & 3)) * 2 + e;
  const char* xg = (const char*)(blk ? xB : xA) + (size_t)b * SQ * DM * 2;
  const char* wqg = (const char*)(blk ? wqB : wqA) + (size_t)h * DH * 2 + (size_t)r * 16 * 8192;
  const char* kg = (const char*)(blk ? kB : kA) + ((size_t)b * NH + h) * SK * DH * 2 + r * 4096;
  const char* vg = (const char*)(blk ? vB : vA) + ((size_t)b * NH + h) * SK * DH * 2 + r * 4096;
  const unsigned ldsh = lds0 + e * (10 * TILE);
  const int rbg = w < 4 ? 3 * r : 6 + 2 * r;
  char* ob = (char*)ows + ((((size_t)(blk * NB + b) * 10 + rbg) * NH + h) * 4) * 1024 + lane * 16;
  if (w < 4) attn_body<3>(xg, wqg, kg, vg, ob, ldsh, r, 16 * rbg, lane); else attn_body<2>(xg, wqg, kg, vg, ob, ldsh, r, 16 * rbg, lane);
}

constexpr int K2_NBUF = 4;
constexpr int K2_LDS = 3 * 4 * 10240 * 1;
__global__ __launch_bounds__(256, 1)
void oproj_kernel(const __hip_bfloat16* __restrict__ ows, const __hip_bfloat16* __restrict__ woA, const __hip_bfloat16* __restrict__ woB,
                  __hip_bfloat16* __restrict__ out) {
  __shared__ __attribute__((aligned(16))) char smem[K2_LDS];
  const unsigned lds0 = (unsigned)(size_t)smem;
  const int tid = threadIdx.x, lane = tid & 63;
  const int w = __builtin_amdgcn_readfirstlane(tid >> 6);
  const int li = lane & 15, g = lane >> 4, q4 = (lane >> 2) & 3, p4 = lane & 3;
  const int idx = blockIdx.x, xcd = idx & 7, r = idx >> 3;
  const int nt = r & 7, bb = (r >> 3) * 8 + xcd;
  const int blk = bb >> 4;
  const char* og = (const char*)ows + (size_t)bb * 10 * 128 * 1024 + (size_t)w * 32 * 1024;
  const char* wog = (const char*)(blk ? woB : woA) + (size_t)(w * 1024) * 1024 + nt * 128;
  const unsigned rowl = lane >> 3, pos = lane & 7;
  const unsigned voffw = rowl * 1024 + (((((pos >> 1) ^ ((rowl >> 1) & 3)) << 1) | (pos & 1)) * 16);
  const unsigned wbase = lds0 + w * (K2_NBUF * 4096);
  const unsigned rr = 4 * g + q4;
  const unsigned trb = wbase + rr * 128 + ((rr >> 1) & 3) * 32 + p4 * 8;
  const unsigned ovoff = lane * 16;

  auto issueWo = [&](int sl, int ks) {
    const unsigned lb = wbase + (sl & (K2_NBUF - 1)) * 4096;
    const char* gb = wog + (size_t)ks * 32 * 1024;
#pragma unroll
    for (int pc = 0; pc < 4; ++pc) dma1(lb + pc * 1024, voffw, gb + pc * 8192);
  };
  f32x4 acc[10][4];
#pragma unroll
  for (int rb = 0; rb < 10; ++rb)
#pragma unroll
    for (int nb = 0; nb < 4; ++nb) acc[rb][nb] = f32x4{0.f, 0.f, 0.f, 0.f};
  s16x8 of[2][10];
  auto loadO = [&](s16x8 (&dst)[10], int ks) {
#pragma unroll
    for (int rb = 0; rb < 10; ++rb) dst[rb] = gload16<0>(ovoff, og + (size_t)rb * 131072 + (size_t)ks * 1024);
  };
#define PIN10(a) asm volatile("" : "+v"(a[0]), "+v"(a[1]), "+v"(a[2]), "+v"(a[3]), "+v"(a[4]), "+v"(a[5]), "+v"(a[6]), "+v"(a[7]), "+v"(a[8]), "+v"(a[9]))
  auto compute = [&](s16x8 (&src)[10], int ks) {
    const unsigned a0 = trb + (ks & (K2_NBUF - 1)) * 4096;
#pragma unroll
    for (int nb = 0; nb < 4; ++nb) {
      const unsigned a = a0 ^ (nb << 5);
      const s16x8 wf = cat8(lds_tr(a), lds_tr(a + 2048));
#pragma unroll
      for (int rb = 0; rb < 10; ++rb) acc[rb][nb] = mfma16(wf, src[rb], acc[rb][nb]);
    }
  };
  issueWo(0, 0); issueWo(1, 1); loadO(of[0], 0);
  for (int ks = 0; ks < 32; ks += 2) {
    issueWo(ks + 2, ks + 2 < 32 ? ks + 2 : 31);
    loadO(of[1], ks + 1);
    WAITV(14); PIN10(of[0]);
    compute(of[0], ks);
    issueWo(ks + 3, ks + 3 < 32 ? ks + 3 : 31);
    loadO(of[0], ks + 2 < 32 ? ks + 2 : 31);
    WAITV(14); PIN10(of[1]);
    compute(of[1], ks + 1);
  }
  WAITV(0);
  __syncthreads();
#pragma unroll
  for (int nb = 0; nb < 4; ++nb) {
    if (nb != w) {
      const int sl = (w - nb - 1) & 3;
#pragma unroll
      for (int rb = 0; rb < 10; ++rb) *(lds_f32x4_ptr)(lds0 + ((nb * 3 + sl) * 10 + rb) * 1024 + lane * 16) = acc[rb][nb];
    }
  }
  __syncthreads();
  char* outp = (char*)out + (size_t)(bb & 15) * SQ * 2048 + blk * 1024 + nt * 128 + w * 32 + g * 8;
#pragma unroll
  for (int rb = 0; rb < 10; ++rb) {
    f32x4 s;
    if (w == 0) s = acc[rb][0]; else if (w == 1) s = acc[rb][1]; else if (w == 2) s = acc[rb][2]; else s = acc[rb][3];
#pragma unroll
    for (int sl = 0; sl < 3; ++sl) s += *(lds_f32x4_ptr)(lds0 + ((w * 3 + sl) * 10 + rb) * 1024 + lane * 16);
    uint2 o2; o2.x = cvtpk(s[0], s[1]); o2.y = cvtpk(s[2], s[3]);
    *(uint2*)(outp + (size_t)(16 * rb + li) * 2048) = o2;
  }
}

extern "C" void kernel_launch(void* const* d_in, const int* in_sizes, int n_in,
                              void* d_out, int out_size, void* d_ws, size_t ws_size,
                              hipStream_t stream) {
  const __hip_bfloat16* xA = (const __hip_bfloat16*)d_in[0];
  const __hip_bfloat16* kA = (const __hip_bfloat16*)d_in[1];
  const __hip_bfloat16* vA = (const __hip_bfloat16*)d_in[2];
  const __hip_bfloat16* wqA = (const __hip_bfloat16*)d_in[3];
  const __hip_bfloat16* woA = (const __hip_bfloat16*)d_in[4];
  const __hip_bfloat16* xB = (const __hip_bfloat16*)d_in[5];
  const __hip_bfloat16* kB = (const __hip_bfloat16*)d_in[6];
  const __hip_bfloat16* vB = (const __hip_bfloat16*)d_in[7];
  const __hip_bfloat16* wqB = (const __hip_bfloat16*)d_in[8];
  const __hip_bfloat16* woB = (const __hip_bfloat16*)d_in[9];
  __hip_bfloat16* ows = (__hip_bfloat16*)d_ws;
  attn_kernel<<<512, 512, 0, stream>>>(xA, kA, vA, wqA, xB, kB, vB, wqB, ows);
  oproj_kernel<<<256, 256, 0, stream>>>(ows, woA, woB, (__hip_bfloat16*)d_out);
}
```

```cpp
#include <hip/hip_runtime.h>
#include <hip/hip_bf16.h>
#include <utility>
#include <type_traits>

typedef __attribute__((ext_vector_type(8))) short s16x8;
typedef __attribute__((ext_vector_type(4))) short s16x4;
typedef __attribute__((ext_vector_type(4))) float f32x4;
typedef __attribute__((ext_vector_type(2))) float f32x2;
typedef __attribute__((ext_vector_type(2))) __bf16 bf16x2;
typedef __attribute__((ext_vector_type(4))) unsigned u32x4;
typedef __attribute__((address_space(3))) s16x4* lds_s16x4_ptr;
typedef __attribute__((address_space(3))) s16x8* lds_s16x8_ptr;
typedef __attribute__((address_space(3))) f32x4* lds_f32x4_ptr;

#define DI __device__ __forceinline__

constexpr int NB = 16, SQ = 160, DM = 512, NH = 32, SK = 1024, DH = 128;
constexpr int NS = 5;
constexpr int SLOT = 16384;
constexpr float QSCALE = 0.088388347648318440f * 1.4426950408889634f;
constexpr float THR = 8.0f;

DI unsigned cvtpk(float lo, float hi) { f32x2 v = {lo, hi}; bf16x2 b = __builtin_convertvector(v, bf16x2); return __builtin_bit_cast(unsigned, b); }
DI s16x8 pack8(f32x4 a, f32x4 b) { u32x4 w = {cvtpk(a[0], a[1]), cvtpk(a[2], a[3]), cvtpk(b[0], b[1]), cvtpk(b[2], b[3])}; return __builtin_bit_cast(s16x8, w); }
DI float max3f(float a, float b, float c) { float r; asm("v_max3_f32 %0, %1, %2, %3" : "=v"(r) : "v"(a), "v"(b), "v"(c)); return r; }
DI float max2f(float a, float b) { float r; asm("v_max_f32_e32 %0, %1, %2" : "=v"(r) : "v"(a), "v"(b)); return r; }
DI f32x4 mfma16(s16x8 a, s16x8 b, f32x4 c) { return __builtin_amdgcn_mfma_f32_16x16x32_bf16(a, b, c, 0, 0, 0); }
DI s16x8 lds_b128(unsigned addr) { return *(lds_s16x8_ptr)(addr); }
DI s16x4 lds_tr(unsigned addr) { return __builtin_amdgcn_ds_read_tr16_b64_v4i16((lds_s16x4_ptr)(addr)); }
DI s16x8 cat8(s16x4 a, s16x4 b) { return __builtin_shufflevector(a, b, 0, 1, 2, 3, 4, 5, 6, 7); }

DI void dma4(unsigned ldsaddr, unsigned v0, unsigned v1, unsigned v2, unsigned v3, const void* gbase) {
  asm volatile("s_mov_b32 m0, %0\n\ts_nop 0\n\t"
               "global_load_lds_dwordx4 %1, %5 nt\n\t"
               "global_load_lds_dwordx4 %2, %5 offset:1024 nt\n\t"
               "global_load_lds_dwordx4 %3, %5 offset:2048 nt\n\t"
               "global_load_lds_dwordx4 %4, %5 offset:3072 nt"
               :: "s"(ldsaddr), "v"(v0), "v"(v1), "v"(v2), "v"(v3), "s"(gbase) : "memory");
}
DI void dma1(unsigned ldsaddr, unsigned v0, const void* gbase) {
  asm volatile("s_mov_b32 m0, %0\n\ts_nop 0\n\t"
               "global_load_lds_dwordx4 %1, %2"
               :: "s"(ldsaddr), "v"(v0), "s"(gbase) : "memory");
}
template <int OFF> DI s16x8 gload16(unsigned voff, const void* gbase) {
  s16x8 r; asm volatile("global_load_dwordx4 %0, %1, %2 offset:%3" : "=v"(r) : "v"(voff), "s"(gbase), "i"(OFF) : "memory"); return r;
}
#define WAITV(n) asm volatile("s_waitcnt vmcnt(" #n ")" ::: "memory")
#define BARRIER() asm volatile("s_barrier" ::: "memory")
#define PIN5(a) asm volatile("" : "+v"(a[0]), "+v"(a[1]), "+v"(a[2]), "+v"(a[3]), "+v"(a[4]))

#define ACLOB "a0","a1","a2","a3","a4","a5","a6","a7","a8","a9","a10","a11","a12","a13","a14","a15","a16","a17","a18","a19","a20","a21","a22","a23","a24","a25","a26","a27","a28","a29","a30","a31","a32","a33","a34","a35","a36","a37","a38","a39","a40","a41","a42","a43","a44","a45","a46","a47","a48","a49","a50","a51","a52","a53","a54","a55","a56","a57","a58","a59","a60","a61","a62","a63","a64","a65","a66","a67","a68","a69","a70","a71","a72","a73","a74","a75","a76","a77","a78","a79","a80","a81","a82","a83","a84","a85","a86","a87","a88","a89","a90","a91","a92","a93","a94","a95","a96","a97","a98","a99","a100","a101","a102","a103","a104","a105","a106","a107","a108","a109","a110","a111","a112","a113","a114","a115","a116","a117","a118","a119","a120","a121","a122","a123","a124","a125","a126","a127"
#define SB() __builtin_amdgcn_sched_barrier(0)
template <int... I, class F> DI void sfor_impl(std::integer_sequence<int, I...>, F&& f) { (f(std::integral_constant<int, I>{}), ...); }
template <int N, class F> DI void sfor(F&& f) { sfor_impl(std::make_integer_sequence<int, N>{}, f); }
template <int N> using IC = std::integral_constant<int, N>;
constexpr int AO(int rb, int db) { return (rb * 8 + db) * 4; }
constexpr int AQ(int f) { return 96 + f * 4; }
template <int A> DI void mfma_acc(s16x8 a, s16x8 b) {
  asm volatile("v_mfma_f32_16x16x32_bf16 a[%c2:%c3], %0, %1, a[%c2:%c3]" :: "v"(a), "v"(b), "i"(A), "i"(A + 3));
}
template <int A> DI void mfma_qk0(f32x4& S, s16x8 kf, const f32x4& nm) {
  asm volatile("v_mfma_f32_16x16x32_bf16 %0, %1, a[%c3:%c4], %2" : "=&v"(S) : "v"(kf), "v"(nm), "i"(A), "i"(A + 3));
}
template <int A> DI void mfma_qk(f32x4& S, s16x8 kf) {
  asm volatile("v_mfma_f32_16x16x32_bf16 %0, %1, a[%c2:%c3], %0" : "+v"(S) : "v"(kf), "i"(A), "i"(A + 3));
}
DI void mfma_v0(f32x4& D, s16x8 a, s16x8 b, const f32x4& c) { asm volatile("v_mfma_f32_16x16x32_bf16 %0, %1, %2, %3" : "=&v"(D) : "v"(a), "v"(b), "v"(c)); }
DI void mfma_v(f32x4& D, s16x8 a, s16x8 b) { asm volatile("v_mfma_f32_16x16x32_bf16 %0, %1, %2, %0" : "+v"(D) : "v"(a), "v"(b)); }
template <int A> DI float acc_rd() { float r; asm volatile("v_accvgpr_read_b32 %0, a[%c1]" : "=v"(r) : "i"(A)); return r; }
template <int A> DI void acc_wr(unsigned v) { asm volatile("v_accvgpr_write_b32 a[%c1], %0" :: "v"(v), "i"(A)); }
template <int A> DI void acc_zero4() { asm volatile("v_accvgpr_write_b32 a[%c0], 0\n\tv_accvgpr_write_b32 a[%c1], 0\n\tv_accvgpr_write_b32 a[%c2], 0\n\tv_accvgpr_write_b32 a[%c3], 0" :: "i"(A), "i"(A + 1), "i"(A + 2), "i"(A + 3)); }
template <int A> DI void acc_scale(float f) { float t; asm volatile("v_accvgpr_read_b32 %0, a[%c2]\n\tv_mul_f32 %0, %0, %1\n\tv_accvgpr_write_b32 a[%c2], %0" : "=&v"(t) : "v"(f), "i"(A)); }
DI float fadd_s(float a, float b) { float r; asm("v_add_f32_e32 %0, %1, %2" : "=v"(r) : "v"(a), "v"(b)); return r; }
template <int LOFF> DI void dma4_nt(unsigned ldsw, unsigned v0, unsigned v1, unsigned v2, unsigned v3, const void* gbase) {
  asm volatile("s_add_u32 m0, %0, %6\n\ts_nop 0\n\t"
               "global_load_lds_dwordx4 %1, %5 nt\n\t"
               "global_load_lds_dwordx4 %2, %5 offset:1024 nt\n\t"
               "global_load_lds_dwordx4 %3, %5 offset:2048 nt\n\t"
               "global_load_lds_dwordx4 %4, %5 offset:3072 nt"
               :: "s"(ldsw), "v"(v0), "v"(v1), "v"(v2), "v"(v3), "s"(gbase), "i"(LOFF) : "memory", "scc");
}
template <int LOFF> DI void dma_m0(unsigned ldsw, unsigned v0, const void* gbase) {
  asm volatile("s_add_u32 m0, %0, %3\n\ts_nop 0\n\tglobal_load_lds_dwordx4 %1, %2" :: "s"(ldsw), "v"(v0), "s"(gbase), "i"(LOFF) : "memory", "scc");
}
#define TS(v) do { } while (0)
constexpr int TILE = 8192;
constexpr int XREG = 120 * 1024, XCH = 20480;

template <int NRB>
DI void attn_body(const char* xg, const char* wqg, const char* kg, const char* vg, char* ob, const unsigned lds0, const unsigned ldsh, const int r, const int bw, const int row0, const int lane
                  ) {
  constexpr bool SMALL = NRB == 2;
  const int li = lane & 15, g = lane >> 4, q4 = (lane >> 2) & 3, p4 = lane & 3;
  const unsigned ldsw = ldsh + r * 4096;
  unsigned voffK[4], voffV[2], voffW[2];
#pragma unroll
  for (int n = 0; n < 4; ++n) voffK[n] = g * 256 + (((li ^ g) ^ (4 * n)) * 16);
#pragma unroll
  for (int n = 0; n < 2; ++n) voffV[n] = g * 256 + ((li ^ (g << 1) ^ (n << 3)) * 16);
#pragma unroll
  for (int n = 0; n < 2; ++n) voffW[n] = g * 8192 + ((li ^ ((g | (n << 2)) << 1)) * 16);
  unsigned kofs[4], wofs[4], vofs[8];
#pragma unroll
  for (int ks = 0; ks < 4; ++ks) kofs[ks] = ldsh + li * 256 + (((4 * ks + g) ^ li) * 16);
  const unsigned r7 = 4 * (g & 1) + q4;
#pragma unroll
  for (int db = 0; db < 8; ++db) vofs[db] = ldsh + (4 * g + q4) * 256 + (r7 ^ db) * 32 + (p4 >> 1) * 16 + (p4 & 1) * 8;
#pragma unroll
  for (int t = 0; t < 4; ++t) wofs[t] = ldsh + 4 * TILE + (8 * g + q4) * 256 + (((4 * t + p4) ^ (2 * q4 | ((g & 1) << 3))) * 16);
  unsigned voffX[2], xofs[2];
#pragma unroll
  for (int n = 0; n < 2; ++n) voffX[n] = (lane >> 3) * 1024 + (((lane & 7) ^ (4 * ((n + bw) & 1) + g)) * 16);
#pragma unroll
  for (int kk = 0; kk < 2; ++kk) xofs[kk] = lds0 + XREG + (row0 + li) * 128 + (((4 * kk + g) ^ (li >> 1)) * 16);

  unsigned long long ts0 = 0, ts1 = 0, ts3 = 0, ts4 = 0, tw0 = 0, tw1 = 0, twsum = 0, tqw = 0;
  (void)ts0; (void)ts1; (void)ts3; (void)ts4; (void)tw0; (void)tw1; (void)twsum; (void)tqw;
  TS(ts0);

  auto tileW = [&](auto N) { constexpr int n = decltype(N)::value;
    sfor<4>([&](auto PC) { constexpr int pc = decltype(PC)::value;
      dma_m0<(4 + n % 3) * TILE + pc * 1024>(ldsw, voffW[(pc >> 1) & 1], wqg + (size_t)(32 * n) * 8192 + (size_t)pc * 32768); }); };
  auto tileK = [&](auto SL, const char* gb) { dma4_nt<decltype(SL)::value * TILE>(ldsw, voffK[0], voffK[1], voffK[2], voffK[3], gb); };
  auto tileV = [&](auto SL, const char* gb) { dma4_nt<decltype(SL)::value * TILE>(ldsw, voffV[0], voffV[1], voffV[0], voffV[1], gb); };

  const unsigned ldsx = lds0 + XREG + bw * 5120;
  const char* xgw = xg + (size_t)bw * 40 * 1024;
  auto chunkX = [&](auto C) { constexpr int c = decltype(C)::value;
    sfor<5>([&](auto PC) { constexpr int pc = decltype(PC)::value;
      dma_m0<(c & 1) * XCH + pc * 1024>(ldsx, voffX[pc & 1], xgw + (size_t)pc * 8192 + c * 128); }); };
  if constexpr (SMALL) { tileW(IC<0>{}); tileW(IC<1>{}); } else { chunkX(IC<0>{}); chunkX(IC<1>{}); }
  asm volatile("" ::: ACLOB);
  sfor<NRB * 8>([&](auto I) { acc_zero4<4 * decltype(I)::value>(); });
  auto qstep = [&](auto N, auto WSC) {
    constexpr int n = decltype(N)::value;
    constexpr int WN = SMALL ? decltype(WSC)::value : (n == 0 ? 5 : (n & 1) ? 63 : 0);
    TS(tw0);
    asm volatile("s_waitcnt vmcnt(%0)" :: "i"(WN) : "memory");
    BARRIER();
    TS(tw1); tqw += tw1 - tw0;
    s16x8 wf[8], xf[NRB];
#pragma unroll
    for (int rb = 0; rb < NRB; ++rb) xf[rb] = lds_b128(xofs[n & 1] + ((n >> 1) & 1) * XCH + rb * 2048);
#pragma unroll
    for (int i = 0; i < 8; ++i) { const unsigned a = wofs[i >> 1] + (n % 3) * TILE + (i & 1) * 8; wf[i] = cat8(lds_tr(a), lds_tr(a + 1024)); }
    SB();
    if constexpr (SMALL) {
      if constexpr (n + 2 < 16) tileW(IC<n + 2>{});
      if constexpr (n < 4) { if constexpr ((n & 1) == 0) tileK(IC<n>{}, kg + (size_t)(n / 2) * 8192); else tileV(IC<n>{}, vg + (size_t)(n / 2) * 8192); }
      if constexpr (n == 14) tileV(IC<5>{}, vg + (size_t)2 * 8192);
    } else {
      if constexpr ((n & 1) == 0 && n >= 2 && n / 2 + 1 < 8) chunkX(IC<n / 2 + 1>{});
    }
    SB();
    sfor<8 * NRB>([&](auto M) { constexpr int m = decltype(M)::value, d = m / NRB, rb = m % NRB; mfma_acc<AO(rb, d)>(wf[d], xf[rb]); });
  };
  qstep(IC<0>{}, IC<4>{}); qstep(IC<1>{}, IC<8>{}); qstep(IC<2>{}, IC<12>{}); qstep(IC<3>{}, IC<12>{});
  qstep(IC<4>{}, IC<12>{}); qstep(IC<5>{}, IC<8>{}); qstep(IC<6>{}, IC<4>{}); qstep(IC<7>{}, IC<4>{});
  qstep(IC<8>{}, IC<4>{}); qstep(IC<9>{}, IC<4>{}); qstep(IC<10>{}, IC<4>{}); qstep(IC<11>{}, IC<4>{});
  qstep(IC<12>{}, IC<4>{}); qstep(IC<13>{}, IC<4>{}); qstep(IC<14>{}, IC<4>{}); qstep(IC<15>{}, IC<4>{});
  BARRIER();
  if constexpr (SMALL) tileK(IC<4>{}, kg + (size_t)2 * 8192);
  asm volatile("s_nop 7\n\ts_nop 7" ::: "memory");
  s16x8 Qv[4];
  sfor<NRB>([&](auto R) { sfor<4>([&](auto Tt) {
    constexpr int rb = decltype(R)::value, t = decltype(Tt)::value, f = rb * 4 + t;
    float v[8];
    sfor<8>([&](auto E) { v[decltype(E)::value] = acc_rd<AO(rb, 2 * t) + decltype(E)::value>() * QSCALE; });
    if constexpr (f < 8) sfor<4>([&](auto E) { constexpr int e = decltype(E)::value; acc_wr<AQ(f) + e>(cvtpk(v[2 * e], v[2 * e + 1])); });
    else { u32x4 q = {cvtpk(v[0], v[1]), cvtpk(v[2], v[3]), cvtpk(v[4], v[5]), cvtpk(v[6], v[7])}; Qv[f - 8] = __builtin_bit_cast(s16x8, q); }
  }); });
  sfor<NRB * 8>([&](auto I) { acc_zero4<4 * decltype(I)::value>(); });

  f32x4 NM[NRB], S[NRB][2];
  float lsum[NRB];
  unsigned Pw[NRB][4];
#pragma unroll
  for (int rb = 0; rb < NRB; ++rb) { lsum[rb] = 0.f; NM[rb] = f32x4{0.f, 0.f, 0.f, 0.f}; }
  auto rowmax = [&](const f32x4 (&Sr)[2]) {
    float t = max3f(Sr[0][0], Sr[0][1], Sr[0][2]);
    t = max3f(t, Sr[0][3], Sr[1][0]); t = max3f(t, Sr[1][1], Sr[1][2]); t = max2f(t, Sr[1][3]);
    return t;
  };
  auto xlane_max = [&](float t) { t = fmaxf(t, __shfl_xor(t, 16)); t = fmaxf(t, __shfl_xor(t, 32)); return t; };
  auto sm_item = [&](int rb, int k) {
    if (k < 8) { float t = __builtin_amdgcn_exp2f(S[rb][k >> 2][k & 3]); asm volatile("" : "+v"(t)); S[rb][k >> 2][k & 3] = t; }
    else if (k < 16) { float t = fadd_s(lsum[rb], S[rb][(k - 8) >> 2][(k - 8) & 3]); asm volatile("" : "+v"(t)); lsum[rb] = t; }
    else { const int c = k - 16; unsigned t = cvtpk(S[rb][c >> 1][(c & 1) * 2], S[rb][c >> 1][(c & 1) * 2 + 1]); asm volatile("" : "+v"(t)); Pw[rb][c] = t; }
  };
  auto Pf = [&](int rb) { u32x4 v = {Pw[rb][0], Pw[rb][1], Pw[rb][2], Pw[rb][3]}; return __builtin_bit_cast(s16x8, v); };

  TS(ts1);
  auto aiter = [&](int j, auto PHC) {
    constexpr int PH = decltype(PHC)::value;
    constexpr int KS = 2 * PH, VS = 2 * PH + 1, KD = 2 * ((PH + 3) % 4), VD = KD + 1;
    TS(tw0);
    if constexpr (SMALL) { if (j < 30) WAITV(16); else WAITV(0); }
    BARRIER();
    TS(tw1); twsum += tw1 - tw0;
    s16x8 kf[2][4], vf[8];
#pragma unroll
    for (int m = 0; m < 8; ++m) kf[m >> 2][m & 3] = lds_b128(kofs[m & 3] + KS * TILE + (m >> 2) * 4096);
    SB();
    if constexpr (SMALL) { if (j + 3 < 32) { tileK(IC<KD>{}, kg + (size_t)(j + 3) * 8192); tileV(IC<VD>{}, vg + (size_t)(j + 3) * 8192); } }
    SB();
    sfor<8 * NRB>([&](auto M) {
      constexpr int m = decltype(M)::value, g8 = m / NRB, rb = m % NRB, kb = g8 >> 2, ks = g8 & 3, f = rb * 4 + ks;
      if constexpr (f < 8) { if constexpr (ks == 0) mfma_qk0<AQ(f)>(S[rb][kb], kf[kb][ks], NM[rb]); else mfma_qk<AQ(f)>(S[rb][kb], kf[kb][ks]); }
      else { if constexpr (ks == 0) mfma_v0(S[rb][kb], kf[kb][ks], Qv[f - 8], NM[rb]); else mfma_v(S[rb][kb], kf[kb][ks], Qv[f - 8]); }
      if constexpr (rb == NRB - 1) { const unsigned a = vofs[g8] + VS * TILE; vf[g8] = cat8(lds_tr(a), lds_tr(a + 4096)); }
      SB();
    });
    if constexpr (NRB == 3) asm volatile("s_nop 7" : "+v"(S[0][0]), "+v"(S[0][1]), "+v"(S[1][0]), "+v"(S[1][1]), "+v"(S[2][0]), "+v"(S[2][1]));
    else asm volatile("s_nop 7" : "+v"(S[0][0]), "+v"(S[0][1]), "+v"(S[1][0]), "+v"(S[1][1]));
    {
      float ta = max2f(S[0][0][0], S[0][0][1]), tb = max2f(S[0][0][2], S[0][0][3]);
#pragma unroll
      for (int i = 1; i < 2 * NRB; ++i) { ta = max3f(ta, S[i >> 1][i & 1][0], S[i >> 1][i & 1][1]); tb = max3f(tb, S[i >> 1][i & 1][2], S[i >> 1][i & 1][3]); }
      const bool first = j == 0;
      if (__builtin_expect(first || __any(max2f(ta, tb) > THR), 0)) {
        sfor<NRB>([&](auto R) {
          constexpr int rb = decltype(R)::value;
          const float rm = xlane_max(rowmax(S[rb]));
          const float dl = first ? rm : fmaxf(rm, 0.f);
          const float al = first ? 1.f : __builtin_amdgcn_exp2f(-dl);
          S[rb][0] -= dl; S[rb][1] -= dl; NM[rb] -= dl; lsum[rb] *= al;
          sfor<32>([&](auto E) { acc_scale<AO(rb, 0) + decltype(E)::value>(al); });
        });
      }
    }
#pragma unroll
    for (int k = 0; k < 20; ++k) sm_item(0, k);
    asm volatile("s_nop 1" ::: "memory");
    SB();
    sfor<8 * NRB>([&](auto M) {
      constexpr int m = decltype(M)::value, rb = m / 8, db = m % 8;
      mfma_acc<AO(rb, db)>(vf[db], Pf(rb));
      if constexpr (rb + 1 < NRB) {
        constexpr int k0 = db < 4 ? 2 * db : 8 + 3 * (db - 4), k1 = db < 4 ? 2 * db + 2 : 8 + 3 * (db - 3);
#pragma unroll
        for (int k = k0; k < k1; ++k) sm_item(rb + 1, k);
        if constexpr (db == 7) asm volatile("s_nop 1" ::: "memory");
      }
      SB();
    });
  };
  for (int j = 0; j < 32; j += 4) { aiter(j, IC<0>{}); aiter(j + 1, IC<1>{}); aiter(j + 2, IC<2>{}); aiter(j + 3, IC<3>{}); }

  TS(ts3);
  asm volatile("s_nop 7\n\ts_nop 7" ::: "memory");
  sfor<NRB>([&](auto R) {
    constexpr int rb = decltype(R)::value;
    float l = lsum[rb]; l += __shfl_xor(l, 16); l += __shfl_xor(l, 32);
    const float il = __builtin_amdgcn_rcpf(l);
    sfor<4>([&](auto Tt) {
      constexpr int t = decltype(Tt)::value;
      float v[8];
      sfor<8>([&](auto E) { v[decltype(E)::value] = acc_rd<AO(rb, 2 * t) + decltype(E)::value>() * il; });
      u32x4 o = {cvtpk(v[0], v[1]), cvtpk(v[2], v[3]), cvtpk(v[4], v[5]), cvtpk(v[6], v[7])};
      *(u32x4*)(ob + (size_t)rb * (NH * 4 * 1024) + t * 1024) = o;
    });
  });
}

__global__ __launch_bounds__(512, 2)
void attn_kernel(const __hip_bfloat16* __restrict__ xA, const __hip_bfloat16* __restrict__ kA, const __hip_bfloat16* __restrict__ vA, const __hip_bfloat16* __restrict__ wqA,
                 const __hip_bfloat16* __restrict__ xB, const __hip_bfloat16* __restrict__ kB, const __hip_bfloat16* __restrict__ vB, const __hip_bfloat16* __restrict__ wqB,
                 __hip_bfloat16* __restrict__ ows) {
  __shared__ __attribute__((aligned(16))) char smem[20 * TILE];
  const unsigned lds0 = (unsigned)(size_t)smem;
  const int tid = threadIdx.x, lane = tid & 63;
  const int w = __builtin_amdgcn_readfirstlane(tid >> 6);
  const int e = w & 1, r = (w >> 1) & 1;
  const int idx = blockIdx.x, xcd = idx & 7, j0 = idx >> 3, blk = j0 >> 5, jj = j0 & 31;
  const int b = (xcd >> 2) * 8 + (jj >> 2), h = ((xcd & 3) * 4 + (jj & 3)) * 2 + e;
  const char* xg = (const char*)(blk ? xB : xA) + (size_t)b * SQ * DM * 2;
  const char* wqg = (const char*)(blk ? wqB : wqA) + (size_t)h * DH * 2 + (size_t)r * 16 * 8192;
  const char* kg = (const char*)(blk ? kB : kA) + ((size_t)b * NH + h) * SK * DH * 2 + r * 4096;
  const char* vg = (const char*)(blk ? vB : vA) + ((size_t)b * NH + h) * SK * DH * 2 + r * 4096;
  const unsigned ldsh = lds0 + e * (8 * TILE);
  const int rbg = w < 4 ? 3 * r : 6 + 2 * r;
  char* ob = (char*)ows + ((((size_t)(blk * NB + b) * 10 + rbg) * NH + h) * 4) * 1024 + lane * 16;
  if (w < 4) attn_body<3>(xg, wqg, kg, vg, ob, lds0, ldsh, r, w & 3, 16 * rbg, lane); else attn_body<2>(xg, wqg, kg, vg, ob, lds0, ldsh, r, w & 3, 16 * rbg, lane);
}

constexpr int K2_NBUF = 4;
constexpr int K2_LDS = 3 * 4 * 10240 * 1;
__global__ __launch_bounds__(256, 1)
void oproj_kernel(const __hip_bfloat16* __restrict__ ows, const __hip_bfloat16* __restrict__ woA, const __hip_bfloat16* __restrict__ woB,
                  __hip_bfloat16* __restrict__ out) {
  __shared__ __attribute__((aligned(16))) char smem[K2_LDS];
  const unsigned lds0 = (unsigned)(size_t)smem;
  const int tid = threadIdx.x, lane = tid & 63;
  const int w = __builtin_amdgcn_readfirstlane(tid >> 6);
  const int li = lane & 15, g = lane >> 4, q4 = (lane >> 2) & 3, p4 = lane & 3;
  const int idx = blockIdx.x, xcd = idx & 7, r = idx >> 3;
  const int nt = r & 7, bb = (r >> 3) * 8 + xcd;
  const int blk = bb >> 4;
  const char* og = (const char*)ows + (size_t)bb * 10 * 128 * 1024 + (size_t)w * 32 * 1024;
  const char* wog = (const char*)(blk ? woB : woA) + (size_t)(w * 1024) * 1024 + nt * 128;
  const unsigned rowl = lane >> 3, pos = lane & 7;
  const unsigned voffw = rowl * 1024 + (((((pos >> 1) ^ ((rowl >> 1) & 3)) << 1) | (pos & 1)) * 16);
  const unsigned wbase = lds0 + w * (K2_NBUF * 4096);
  const unsigned rr = 4 * g + q4;
  const unsigned trb = wbase + rr * 128 + ((rr >> 1) & 3) * 32 + p4 * 8;
  const unsigned ovoff = lane * 16;

  auto issueWo = [&](int sl, int ks) {
    const unsigned lb = wbase + (sl & (K2_NBUF - 1)) * 4096;
    const char* gb = wog + (size_t)ks * 32 * 1024;
#pragma unroll
    for (int pc = 0; pc < 4; ++pc) dma1(lb + pc * 1024, voffw, gb + pc * 8192);
  };
  f32x4 acc[10][4];
#pragma unroll
  for (int rb = 0; rb < 10; ++rb)
#pragma unroll
    for (int nb = 0; nb < 4; ++nb) acc[rb][nb] = f32x4{0.f, 0.f, 0.f, 0.f};
  s16x8 of[2][10];
  auto loadO = [&](s16x8 (&dst)[10], int ks) {
#pragma unroll
    for (int rb = 0; rb < 10; ++rb) dst[rb] = gload16<0>(ovoff, og + (size_t)rb * 131072 + (size_t)ks * 1024);
  };
#define PIN10(a) asm volatile("" : "+v"(a[0]), "+v"(a[1]), "+v"(a[2]), "+v"(a[3]), "+v"(a[4]), "+v"(a[5]), "+v"(a[6]), "+v"(a[7]), "+v"(a[8]), "+v"(a[9]))
  auto compute = [&](s16x8 (&src)[10], int ks) {
    const unsigned a0 = trb + (ks & (K2_NBUF - 1)) * 4096;
#pragma unroll
    for (int nb = 0; nb < 4; ++nb) {
      const unsigned a = a0 ^ (nb << 5);
      const s16x8 wf = cat8(lds_tr(a), lds_tr(a + 2048));
#pragma unroll
      for (int rb = 0; rb < 10; ++rb) acc[rb][nb] = mfma16(wf, src[rb], acc[rb][nb]);
    }
  };
  issueWo(0, 0); issueWo(1, 1); loadO(of[0], 0);
  for (int ks = 0; ks < 32; ks += 2) {
    issueWo(ks + 2, ks + 2 < 32 ? ks + 2 : 31);
    loadO(of[1], ks + 1);
    WAITV(14); PIN10(of[0]);
    compute(of[0], ks);
    issueWo(ks + 3, ks + 3 < 32 ? ks + 3 : 31);
    loadO(of[0], ks + 2 < 32 ? ks + 2 : 31);
    WAITV(14); PIN10(of[1]);
    compute(of[1], ks + 1);
  }
  WAITV(0);
  __syncthreads();
#pragma unroll
  for (int nb = 0; nb < 4; ++nb) {
    if (nb != w) {
      const int sl = (w - nb - 1) & 3;
#pragma unroll
      for (int rb = 0; rb < 10; ++rb) *(lds_f32x4_ptr)(lds0 + ((nb * 3 + sl) * 10 + rb) * 1024 + lane * 16) = acc[rb][nb];
    }
  }
  __syncthreads();
  char* outp = (char*)out + (size_t)(bb & 15) * SQ * 2048 + blk * 1024 + nt * 128 + w * 32 + g * 8;
#pragma unroll
  for (int rb = 0; rb < 10; ++rb) {
    f32x4 s;
    if (w == 0) s = acc[rb][0]; else if (w == 1) s = acc[rb][1]; else if (w == 2) s = acc[rb][2]; else s = acc[rb][3];
#pragma unroll
    for (int sl = 0; sl < 3; ++sl) s += *(lds_f32x4_ptr)(lds0 + ((w * 3 + sl) * 10 + rb) * 1024 + lane * 16);
    uint2 o2; o2.x = cvtpk(s[0], s[1]); o2.y = cvtpk(s[2], s[3]);
    *(uint2*)(outp + (size_t)(16 * rb + li) * 2048) = o2;
  }
}

extern "C" void kernel_launch(void* const* d_in, const int* in_sizes, int n_in,
                              void* d_out, int out_size, void* d_ws, size_t ws_size,
                              hipStream_t stream) {
  const __hip_bfloat16* xA = (const __hip_bfloat16*)d_in[0];
  const __hip_bfloat16* kA = (const __hip_bfloat16*)d_in[1];
  const __hip_bfloat16* vA = (const __hip_bfloat16*)d_in[2];
  const __hip_bfloat16* wqA = (const __hip_bfloat16*)d_in[3];
  const __hip_bfloat16* woA = (const __hip_bfloat16*)d_in[4];
  const __hip_bfloat16* xB = (const __hip_bfloat16*)d_in[5];
  const __hip_bfloat16* kB = (const __hip_bfloat16*)d_in[6];
  const __hip_bfloat16* vB = (const __hip_bfloat16*)d_in[7];
  const __hip_bfloat16* wqB = (const __hip_bfloat16*)d_in[8];
  const __hip_bfloat16* woB = (const __hip_bfloat16*)d_in[9];
  __hip_bfloat16* ows = (__hip_bfloat16*)d_ws;
  attn_kernel<<<512, 512, 0, stream>>>(xA, kA, vA, wqA, xB, kB, vB, wqB, ows);
  oproj_kernel<<<256, 256, 0, stream>>>(ows, woA, woB, (__hip_bfloat16*)d_out);
}
```

```cpp
#include <hip/hip_runtime.h>
#include <hip/hip_bf16.h>
#include <utility>
#include <type_traits>

typedef __attribute__((ext_vector_type(8))) short s16x8;
typedef __attribute__((ext_vector_type(4))) short s16x4;
typedef __attribute__((ext_vector_type(4))) float f32x4;
typedef __attribute__((ext_vector_type(2))) float f32x2;
typedef __attribute__((ext_vector_type(2))) __bf16 bf16x2;
typedef __attribute__((ext_vector_type(4))) unsigned u32x4;
typedef __attribute__((address_space(3))) s16x4* lds_s16x4_ptr;
typedef __attribute__((address_space(3))) s16x8* lds_s16x8_ptr;
typedef __attribute__((address_space(3))) f32x4* lds_f32x4_ptr;

#define DI __device__ __forceinline__

constexpr int NB = 16, SQ = 160, DM = 512, NH = 32, SK = 1024, DH = 128;
constexpr int NS = 5;
constexpr int SLOT = 16384;
constexpr float QSCALE = 0.088388347648318440f * 1.4426950408889634f;
constexpr float THR = 8.0f;

DI unsigned cvtpk(float lo, float hi) { f32x2 v = {lo, hi}; bf16x2 b = __builtin_convertvector(v, bf16x2); return __builtin_bit_cast(unsigned, b); }
DI s16x8 pack8(f32x4 a, f32x4 b) { u32x4 w = {cvtpk(a[0], a[1]), cvtpk(a[2], a[3]), cvtpk(b[0], b[1]), cvtpk(b[2], b[3])}; return __builtin_bit_cast(s16x8, w); }
DI float max3f(float a, float b, float c) { float r; asm("v_max3_f32 %0, %1, %2, %3" : "=v"(r) : "v"(a), "v"(b), "v"(c)); return r; }
DI float max2f(float a, float b) { float r; asm("v_max_f32_e32 %0, %1, %2" : "=v"(r) : "v"(a), "v"(b)); return r; }
DI f32x4 mfma16(s16x8 a, s16x8 b, f32x4 c) { return __builtin_amdgcn_mfma_f32_16x16x32_bf16(a, b, c, 0, 0, 0); }
DI s16x8 lds_b128(unsigned addr) { return *(lds_s16x8_ptr)(addr); }
DI s16x4 lds_tr(unsigned addr) { return __builtin_amdgcn_ds_read_tr16_b64_v4i16((lds_s16x4_ptr)(addr)); }
DI s16x8 cat8(s16x4 a, s16x4 b) { return __builtin_shufflevector(a, b, 0, 1, 2, 3, 4, 5, 6, 7); }

DI void dma4(unsigned ldsaddr, unsigned v0, unsigned v1, unsigned v2, unsigned v3, const void* gbase) {
  asm volatile("s_mov_b32 m0, %0\n\ts_nop 0\n\t"
               "global_load_lds_dwordx4 %1, %5 nt\n\t"
               "global_load_lds_dwordx4 %2, %5 offset:1024 nt\n\t"
               "global_load_lds_dwordx4 %3, %5 offset:2048 nt\n\t"
               "global_load_lds_dwordx4 %4, %5 offset:3072 nt"
               :: "s"(ldsaddr), "v"(v0), "v"(v1), "v"(v2), "v"(v3), "s"(gbase) : "memory");
}
DI void dma1(unsigned ldsaddr, unsigned v0, const void* gbase) {
  asm volatile("s_mov_b32 m0, %0\n\ts_nop 0\n\t"
               "global_load_lds_dwordx4 %1, %2"
               :: "s"(ldsaddr), "v"(v0), "s"(gbase) : "memory");
}
template <int OFF> DI s16x8 gload16(unsigned voff, const void* gbase) {
  s16x8 r; asm volatile("global_load_dwordx4 %0, %1, %2 offset:%3" : "=v"(r) : "v"(voff), "s"(gbase), "i"(OFF) : "memory"); return r;
}
#define WAITV(n) asm volatile("s_waitcnt vmcnt(" #n ")" ::: "memory")
#define BARRIER() asm volatile("s_barrier" ::: "memory")
#define PIN5(a) asm volatile("" : "+v"(a[0]), "+v"(a[1]), "+v"(a[2]), "+v"(a[3]), "+v"(a[4]))

#define ACLOB "a0","a1","a2","a3","a4","a5","a6","a7","a8","a9","a10","a11","a12","a13","a14","a15","a16","a17","a18","a19","a20","a21","a22","a23","a24","a25","a26","a27","a28","a29","a30","a31","a32","a33","a34","a35","a36","a37","a38","a39","a40","a41","a42","a43","a44","a45","a46","a47","a48","a49","a50","a51","a52","a53","a54","a55","a56","a57","a58","a59","a60","a61","a62","a63","a64","a65","a66","a67","a68","a69","a70","a71","a72","a73","a74","a75","a76","a77","a78","a79","a80","a81","a82","a83","a84","a85","a86","a87","a88","a89","a90","a91","a92","a93","a94","a95","a96","a97","a98","a99","a100","a101","a102","a103","a104","a105","a106","a107","a108","a109","a110","a111","a112","a113","a114","a115","a116","a117","a118","a119","a120","a121","a122","a123","a124","a125","a126","a127"
#define SB() __builtin_amdgcn_sched_barrier(0)
template <int... I, class F> DI void sfor_impl(std::integer_sequence<int, I...>, F&& f) { (f(std::integral_constant<int, I>{}), ...); }
template <int N, class F> DI void sfor(F&& f) { sfor_impl(std::make_integer_sequence<int, N>{}, f); }
template <int N> using IC = std::integral_constant<int, N>;
constexpr int AO(int rb, int db) { return (rb * 8 + db) * 4; }
constexpr int AQ(int f) { return 96 + f * 4; }
template <int A> DI void mfma_acc(s16x8 a, s16x8 b) {
  asm volatile("v_mfma_f32_16x16x32_bf16 a[%c2:%c3], %0, %1, a[%c2:%c3]" :: "v"(a), "v"(b), "i"(A), "i"(A + 3));
}
template <int A> DI void mfma_qk0(f32x4& S, s16x8 kf, const f32x4& nm) {
  asm volatile("v_mfma_f32_16x16x32_bf16 %0, %1, a[%c3:%c4], %2" : "=&v"(S) : "v"(kf), "v"(nm), "i"(A), "i"(A + 3));
}
template <int A> DI void mfma_qk(f32x4& S, s16x8 kf) {
  asm volatile("v_mfma_f32_16x16x32_bf16 %0, %1, a[%c2:%c3], %0" : "+v"(S) : "v"(kf), "i"(A), "i"(A + 3));
}
DI void mfma_v0(f32x4& D, s16x8 a, s16x8 b, const f32x4& c) { asm volatile("v_mfma_f32_16x16x32_bf16 %0, %1, %2, %3" : "=&v"(D) : "v"(a), "v"(b), "v"(c)); }
DI void mfma_v(f32x4& D, s16x8 a, s16x8 b) { asm volatile("v_mfma_f32_16x16x32_bf16 %0, %1, %2, %0" : "+v"(D) : "v"(a), "v"(b)); }
template <int A> DI float acc_rd() { float r; asm volatile("v_accvgpr_read_b32 %0, a[%c1]" : "=v"(r) : "i"(A)); return r; }
template <int A> DI void acc_wr(unsigned v) { asm volatile("v_accvgpr_write_b32 a[%c1], %0" :: "v"(v), "i"(A)); }
template <int A> DI void acc_zero4() { asm volatile("v_accvgpr_write_b32 a[%c0], 0\n\tv_accvgpr_write_b32 a[%c1], 0\n\tv_accvgpr_write_b32 a[%c2], 0\n\tv_accvgpr_write_b32 a[%c3], 0" :: "i"(A), "i"(A + 1), "i"(A + 2), "i"(A + 3)); }
template <int A> DI void acc_scale(float f) { float t; asm volatile("v_accvgpr_read_b32 %0, a[%c2]\n\tv_mul_f32 %0, %0, %1\n\tv_accvgpr_write_b32 a[%c2], %0" : "=&v"(t) : "v"(f), "i"(A)); }
DI float fadd_s(float a, float b) { float r; asm("v_add_f32_e32 %0, %1, %2" : "=v"(r) : "v"(a), "v"(b)); return r; }
template <int LOFF> DI void dma4_nt(unsigned ldsw, unsigned v0, unsigned v1, unsigned v2, unsigned v3, const void* gbase) {
  asm volatile("s_add_u32 m0, %0, %6\n\ts_nop 0\n\t"
               "global_load_lds_dwordx4 %1, %5 nt\n\t"
               "global_load_lds_dwordx4 %2, %5 offset:1024 nt\n\t"
               "global_load_lds_dwordx4 %3, %5 offset:2048 nt\n\t"
               "global_load_lds_dwordx4 %4, %5 offset:3072 nt"
               :: "s"(ldsw), "v"(v0), "v"(v1), "v"(v2), "v"(v3), "s"(gbase), "i"(LOFF) : "memory", "scc");
}
template <int LOFF> DI void dma_m0(unsigned ldsw, unsigned v0, const void* gbase) {
  asm volatile("s_add_u32 m0, %0, %3\n\ts_nop 0\n\tglobal_load_lds_dwordx4 %1, %2" :: "s"(ldsw), "v"(v0), "s"(gbase), "i"(LOFF) : "memory", "scc");
}
#define TS(v) do { } while (0)
constexpr int TILE = 8192;
constexpr int XREG = 120 * 1024, XCH = 20480;

template <int NRB>
DI void attn_body(const char* xg, const char* wqg, const char* kg, const char* vg, char* ob, const unsigned lds0, const unsigned ldsh, const int r, const int bw, const int row0, const int lane
                  ) {
  constexpr bool SMALL = NRB == 2;
  const int li = lane & 15, g = lane >> 4, q4 = (lane >> 2) & 3, p4 = lane & 3;
  const unsigned ldsw = ldsh + r * 4096;
  unsigned voffK[4], voffV[2], voffW[2];
#pragma unroll
  for (int n = 0; n < 4; ++n) voffK[n] = g * 256 + (((li ^ g) ^ (4 * n)) * 16);
#pragma unroll
  for (int n = 0; n < 2; ++n) voffV[n] = g * 256 + ((li ^ (g << 1) ^ (n << 3)) * 16);
#pragma unroll
  for (int n = 0; n < 2; ++n) voffW[n] = g * 8192 + ((li ^ ((g | (n << 2)) << 1)) * 16);
  unsigned kofs[4], wofs[4], vofs[8];
#pragma unroll
  for (int ks = 0; ks < 4; ++ks) kofs[ks] = ldsh + li * 256 + (((4 * ks + g) ^ li) * 16);
  const unsigned r7 = 4 * (g & 1) + q4;
#pragma unroll
  for (int db = 0; db < 8; ++db) vofs[db] = ldsh + (4 * g + q4) * 256 + (r7 ^ db) * 32 + (p4 >> 1) * 16 + (p4 & 1) * 8;
#pragma unroll
  for (int t = 0; t < 4; ++t) wofs[t] = ldsh + 4 * TILE + (8 * g + q4) * 256 + (((4 * t + p4) ^ (2 * q4 | ((g & 1) << 3))) * 16);
  unsigned voffX[2], xofs[2];
#pragma unroll
  for (int n = 0; n < 2; ++n) voffX[n] = (lane >> 3) * 1024 + (((lane & 7) ^ (4 * ((n + bw) & 1) + g)) * 16);
#pragma unroll
  for (int kk = 0; kk < 2; ++kk) xofs[kk] = lds0 + XREG + (row0 + li) * 128 + (((4 * kk + g) ^ (li >> 1)) * 16);

  unsigned long long ts0 = 0, ts1 = 0, ts3 = 0, ts4 = 0, tw0 = 0, tw1 = 0, twsum = 0, tqw = 0;
  (void)ts0; (void)ts1; (void)ts3; (void)ts4; (void)tw0; (void)tw1; (void)twsum; (void)tqw;
  TS(ts0);

  auto tileW = [&](auto N) { constexpr int n = decltype(N)::value;
    sfor<4>([&](auto PC) { constexpr int pc = decltype(PC)::value;
      dma_m0<(4 + n % 3) * TILE + pc * 1024>(ldsw, voffW[(pc >> 1) & 1], wqg + (size_t)(32 * n) * 8192 + (size_t)pc * 32768); }); };
  auto tileK = [&](auto SL, const char* gb) { dma4_nt<decltype(SL)::value * TILE>(ldsw, voffK[0], voffK[1], voffK[2], voffK[3], gb); };
  auto tileV = [&](auto SL, const char* gb) { dma4_nt<decltype(SL)::value * TILE>(ldsw, voffV[0], voffV[1], voffV[0], voffV[1], gb); };

  const unsigned ldsx = lds0 + XREG + bw * 5120;
  const char* xgw = xg + (size_t)bw * 40 * 1024;
  auto chunkX = [&](auto C) { constexpr int c = decltype(C)::value;
    sfor<5>([&](auto PC) { constexpr int pc = decltype(PC)::value;
      dma_m0<(c & 1) * XCH + pc * 1024>(ldsx, voffX[pc & 1], xgw + (size_t)pc * 8192 + c * 128); }); };
  if constexpr (SMALL) { tileW(IC<0>{}); tileW(IC<1>{}); tileW(IC<2>{}); } else { chunkX(IC<0>{}); chunkX(IC<1>{}); }
  asm volatile("" ::: ACLOB);
  sfor<NRB * 8>([&](auto I) { acc_zero4<4 * decltype(I)::value>(); });
  s16x8 wf[2][8], xf[2][NRB];
  auto wread = [&](auto N, auto I) { constexpr int n = decltype(N)::value, i = decltype(I)::value;
    const unsigned a = wofs[i >> 1] + (n % 3) * TILE + (i & 1) * 8; wf[n & 1][i] = cat8(lds_tr(a), lds_tr(a + 1024)); };
  auto xread = [&](auto N, auto R) { constexpr int n = decltype(N)::value, rb = decltype(R)::value;
    xf[n & 1][rb] = lds_b128(xofs[n & 1] + ((n >> 1) & 1) * XCH + rb * 2048); };
  if constexpr (SMALL) WAITV(8); else WAITV(5);
  BARRIER();
  sfor<NRB>([&](auto R) { xread(IC<0>{}, R); });
  sfor<8>([&](auto I) { wread(IC<0>{}, I); });
  auto qstep = [&](auto N, auto WSC) {
    constexpr int n = decltype(N)::value;
    constexpr int WN = SMALL ? decltype(WSC)::value : ((n & 1) ? 0 : 63);
    TS(tw0);
    asm volatile("s_waitcnt vmcnt(%0) lgkmcnt(0)" :: "i"(WN) : "memory");
    BARRIER();
    TS(tw1); tqw += tw1 - tw0;
    SB();
    if constexpr (SMALL) {
      if constexpr (n + 3 < 16) tileW(IC<n + 3>{});
      if constexpr (n < 4) { if constexpr ((n & 1) == 0) tileK(IC<n>{}, kg + (size_t)(n / 2) * 8192); else tileV(IC<n>{}, vg + (size_t)(n / 2) * 8192); }
      if constexpr (n == 13) tileV(IC<5>{}, vg + (size_t)2 * 8192);
    } else {
      if constexpr ((n & 1) == 1 && (n + 3) / 2 < 8) chunkX(IC<(n + 3) / 2>{});
    }
    SB();
    sfor<8 * NRB>([&](auto M) { constexpr int m = decltype(M)::value, d = m / NRB, rb = m % NRB;
      mfma_acc<AO(rb, d)>(wf[n & 1][d], xf[n & 1][rb]);
      if constexpr (n < 15) {
        if constexpr (m < NRB) xread(IC<n + 1>{}, IC<m>{});
        else if constexpr (m < NRB + 8) wread(IC<n + 1>{}, IC<m - NRB>{});
      }
      SB();
    });
  };
  qstep(IC<0>{}, IC<4>{}); qstep(IC<1>{}, IC<8>{}); qstep(IC<2>{}, IC<12>{}); qstep(IC<3>{}, IC<12>{});
  qstep(IC<4>{}, IC<12>{}); qstep(IC<5>{}, IC<8>{}); qstep(IC<6>{}, IC<4>{}); qstep(IC<7>{}, IC<4>{});
  qstep(IC<8>{}, IC<4>{}); qstep(IC<9>{}, IC<4>{}); qstep(IC<10>{}, IC<4>{}); qstep(IC<11>{}, IC<4>{});
  qstep(IC<12>{}, IC<4>{}); qstep(IC<13>{}, IC<4>{}); qstep(IC<14>{}, IC<4>{}); qstep(IC<15>{}, IC<4>{});
  if constexpr (SMALL) tileK(IC<4>{}, kg + (size_t)2 * 8192);
  asm volatile("s_nop 7\n\ts_nop 7" ::: "memory");
  s16x8 Qv[4];
  sfor<NRB>([&](auto R) { sfor<4>([&](auto Tt) {
    constexpr int rb = decltype(R)::value, t = decltype(Tt)::value, f = rb * 4 + t;
    float v[8];
    sfor<8>([&](auto E) { v[decltype(E)::value] = acc_rd<AO(rb, 2 * t) + decltype(E)::value>() * QSCALE; });
    if constexpr (f < 8) sfor<4>([&](auto E) { constexpr int e = decltype(E)::value; acc_wr<AQ(f) + e>(cvtpk(v[2 * e], v[2 * e + 1])); });
    else { u32x4 q = {cvtpk(v[0], v[1]), cvtpk(v[2], v[3]), cvtpk(v[4], v[5]), cvtpk(v[6], v[7])}; Qv[f - 8] = __builtin_bit_cast(s16x8, q); }
  }); });
  sfor<NRB * 8>([&](auto I) { acc_zero4<4 * decltype(I)::value>(); });

  f32x4 NM[NRB], S[NRB][2];
  float lsum[NRB];
  unsigned Pw[NRB][4];
#pragma unroll
  for (int rb = 0; rb < NRB; ++rb) { lsum[rb] = 0.f; NM[rb] = f32x4{0.f, 0.f, 0.f, 0.f}; }
  auto rowmax = [&](const f32x4 (&Sr)[2]) {
    float t = max3f(Sr[0][0], Sr[0][1], Sr[0][2]);
    t = max3f(t, Sr[0][3], Sr[1][0]); t = max3f(t, Sr[1][1], Sr[1][2]); t = max2f(t, Sr[1][3]);
    return t;
  };
  auto xlane_max = [&](float t) { t = fmaxf(t, __shfl_xor(t, 16)); t = fmaxf(t, __shfl_xor(t, 32)); return t; };
  auto sm_item = [&](int rb, int k) {
    if (k < 8) { float t = __builtin_amdgcn_exp2f(S[rb][k >> 2][k & 3]); asm volatile("" : "+v"(t)); S[rb][k >> 2][k & 3] = t; }
    else if (k < 16) { float t = fadd_s(lsum[rb], S[rb][(k - 8) >> 2][(k - 8) & 3]); asm volatile("" : "+v"(t)); lsum[rb] = t; }
    else { const int c = k - 16; unsigned t = cvtpk(S[rb][c >> 1][(c & 1) * 2], S[rb][c >> 1][(c & 1) * 2 + 1]); asm volatile("" : "+v"(t)); Pw[rb][c] = t; }
  };
  auto Pf = [&](int rb) { u32x4 v = {Pw[rb][0], Pw[rb][1], Pw[rb][2], Pw[rb][3]}; return __builtin_bit_cast(s16x8, v); };

  TS(ts1);
  auto aiter = [&](int j, auto PHC) {
    constexpr int PH = decltype(PHC)::value;
    constexpr int KS = 2 * PH, VS = 2 * PH + 1, KD = 2 * ((PH + 3) % 4), VD = KD + 1;
    TS(tw0);
    if constexpr (SMALL) { if (j < 30) WAITV(16); else WAITV(0); }
    BARRIER();
    TS(tw1); twsum += tw1 - tw0;
    s16x8 kf[2][4], vf[8];
#pragma unroll
    for (int m = 0; m < 8; ++m) kf[m >> 2][m & 3] = lds_b128(kofs[m & 3] + KS * TILE + (m >> 2) * 4096);
    SB();
    if constexpr (SMALL) { if (j + 3 < 32) { tileK(IC<KD>{}, kg + (size_t)(j + 3) * 8192); tileV(IC<VD>{}, vg + (size_t)(j + 3) * 8192); } }
    SB();
    sfor<8 * NRB>([&](auto M) {
      constexpr int m = decltype(M)::value, g8 = m / NRB, rb = m % NRB, kb = g8 >> 2, ks = g8 & 3, f = rb * 4 + ks;
      if constexpr (f < 8) { if constexpr (ks == 0) mfma_qk0<AQ(f)>(S[rb][kb], kf[kb][ks], NM[rb]); else mfma_qk<AQ(f)>(S[rb][kb], kf[kb][ks]); }
      else { if constexpr (ks == 0) mfma_v0(S[rb][kb], kf[kb][ks], Qv[f - 8], NM[rb]); else mfma_v(S[rb][kb], kf[kb][ks], Qv[f - 8]); }
      if constexpr (rb == NRB - 1) { const unsigned a = vofs[g8] + VS * TILE; vf[g8] = cat8(lds_tr(a), lds_tr(a + 4096)); }
      SB();
    });
    if constexpr (NRB == 3) asm volatile("s_nop 7" : "+v"(S[0][0]), "+v"(S[0][1]), "+v"(S[1][0]), "+v"(S[1][1]), "+v"(S[2][0]), "+v"(S[2][1]));
    else asm volatile("s_nop 7" : "+v"(S[0][0]), "+v"(S[0][1]), "+v"(S[1][0]), "+v"(S[1][1]));
    {
      float ta = max2f(S[0][0][0], S[0][0][1]), tb = max2f(S[0][0][2], S[0][0][3]);
#pragma unroll
      for (int i = 1; i < 2 * NRB; ++i) { ta = max3f(ta, S[i >> 1][i & 1][0], S[i >> 1][i & 1][1]); tb = max3f(tb, S[i >> 1][i & 1][2], S[i >> 1][i & 1][3]); }
      const bool first = j == 0;
      if (__builtin_expect(first || __any(max2f(ta, tb) > THR), 0)) {
        sfor<NRB>([&](auto R) {
          constexpr int rb = decltype(R)::value;
          const float rm = xlane_max(rowmax(S[rb]));
          const float dl = first ? rm : fmaxf(rm, 0.f);
          const float al = first ? 1.f : __builtin_amdgcn_exp2f(-dl);
          S[rb][0] -= dl; S[rb][1] -= dl; NM[rb] -= dl; lsum[rb] *= al;
          sfor<32>([&](auto E) { acc_scale<AO(rb, 0) + decltype(E)::value>(al); });
        });
      }
    }
#pragma unroll
    for (int k = 0; k < 20; ++k) sm_item(0, k);
    asm volatile("s_nop 1" ::: "memory");
    SB();
    sfor<8 * NRB>([&](auto M) {
      constexpr int m = decltype(M)::value, rb = m / 8, db = m % 8;
      mfma_acc<AO(rb, db)>(vf[db], Pf(rb));
      if constexpr (rb + 1 < NRB) {
        constexpr int k0 = db < 4 ? 2 * db : 8 + 3 * (db - 4), k1 = db < 4 ? 2 * db + 2 : 8 + 3 * (db - 3);
#pragma unroll
        for (int k = k0; k < k1; ++k) sm_item(rb + 1, k);
        if constexpr (db == 7) asm volatile("s_nop 1" ::: "memory");
      }
      SB();
    });
  };
  for (int j = 0; j < 32; j += 4) { aiter(j, IC<0>{}); aiter(j + 1, IC<1>{}); aiter(j + 2, IC<2>{}); aiter(j + 3, IC<3>{}); }

  TS(ts3);
  asm volatile("s_nop 7\n\ts_nop 7" ::: "memory");
  sfor<NRB>([&](auto R) {
    constexpr int rb = decltype(R)::value;
    float l = lsum[rb]; l += __shfl_xor(l, 16); l += __shfl_xor(l, 32);
    const float il = __builtin_amdgcn_rcpf(l);
    sfor<4>([&](auto Tt) {
      constexpr int t = decltype(Tt)::value;
      float v[8];
      sfor<8>([&](auto E) { v[decltype(E)::value] = acc_rd<AO(rb, 2 * t) + decltype(E)::value>() * il; });
      u32x4 o = {cvtpk(v[0], v[1]), cvtpk(v[2], v[3]), cvtpk(v[4], v[5]), cvtpk(v[6], v[7])};
      *(u32x4*)(ob + (size_t)rb * (NH * 4 * 1024) + t * 1024) = o;
    });
  });
}

__global__ __launch_bounds__(512, 2)
void attn_kernel(const __hip_bfloat16* __restrict__ xA, const __hip_bfloat16* __restrict__ kA, const __hip_bfloat16* __restrict__ vA, const __hip_bfloat16* __restrict__ wqA,
                 const __hip_bfloat16* __restrict__ xB, const __hip_bfloat16* __restrict__ kB, const __hip_bfloat16* __restrict__ vB, const __hip_bfloat16* __restrict__ wqB,
                 __hip_bfloat16* __restrict__ ows) {
  __shared__ __attribute__((aligned(16))) char smem[20 * TILE];
  const unsigned lds0 = (unsigned)(size_t)smem;
  const int tid = threadIdx.x, lane = tid & 63;
  const int w = __builtin_amdgcn_readfirstlane(tid >> 6);
  const int e = w & 1, r = (w >> 1) & 1;
  const int idx = blockIdx.x, xcd = idx & 7, j0 = idx >> 3, blk = j0 >> 5, jj = j0 & 31;
  const int b = (xcd >> 2) * 8 + (jj >> 2), h = ((xcd & 3) * 4 + (jj & 3)) * 2 + e;
  const char* xg = (const char*)(blk ? xB : xA) + (size_t)b * SQ * DM * 2;
  const char* wqg = (const char*)(blk ? wqB : wqA) + (size_t)h * DH * 2 + (size_t)r * 16 * 8192;
  const char* kg = (const char*)(blk ? kB : kA) + ((size_t)b * NH + h) * SK * DH * 2 + r * 4096;
  const char* vg = (const char*)(blk ? vB : vA) + ((size_t)b * NH + h) * SK * DH * 2 + r * 4096;
  const unsigned ldsh = lds0 + e * (8 * TILE);
  const int rbg = w < 4 ? 3 * r : 6 + 2 * r;
  char* ob = (char*)ows + ((((size_t)(blk * NB + b) * 10 + rbg) * NH + h) * 4) * 1024 + lane * 16;
  if (w < 4) attn_body<3>(xg, wqg, kg, vg, ob, lds0, ldsh, r, w & 3, 16 * rbg, lane); else attn_body<2>(xg, wqg, kg, vg, ob, lds0, ldsh, r, w & 3, 16 * rbg, lane);
}

constexpr int K2_LDS = 8 * 15 * 1024;
__global__ __launch_bounds__(512, 2)
void oproj_kernel(const __hip_bfloat16* __restrict__ ows, const __hip_bfloat16* __restrict__ woA, const __hip_bfloat16* __restrict__ woB,
                  __hip_bfloat16* __restrict__ out) {
  __shared__ __attribute__((aligned(16))) char smem[K2_LDS];
  const unsigned lds0 = (unsigned)(size_t)smem;
  const int tid = threadIdx.x, lane = tid & 63;
  const int w = __builtin_amdgcn_readfirstlane(tid >> 6);
  const int rh = w >> 2, kq = w & 3;
  const int li = lane & 15, g = lane >> 4, q4 = (lane >> 2) & 3, p4 = lane & 3;
  const int idx = blockIdx.x, xcd = idx & 7, r = idx >> 3;
  const int nt = r & 7, bb = (r >> 3) * 8 + xcd;
  const int blk = bb >> 4;
  const char* og = (const char*)ows + ((size_t)bb * 10 + rh * 5) * 128 * 1024 + (size_t)kq * 32 * 1024;
  const char* wog = (const char*)(blk ? woB : woA) + (size_t)(kq * 1024 + rh * 16) * 1024 + nt * 128;
  const unsigned rowl = lane >> 3, pos = lane & 7;
  const unsigned voffw = rowl * 1024 + (((((pos >> 1) ^ ((rowl >> 1) & 3)) << 1) | (pos & 1)) * 16);
  const unsigned wbase = lds0 + kq * 16384;
  const unsigned wdst = wbase + rh * 2048;
  const unsigned rr = 4 * g + q4;
  unsigned trb[4];
#pragma unroll
  for (int nb = 0; nb < 4; ++nb) trb[nb] = wbase + rr * 128 + ((((rr >> 1) & 3) ^ nb) * 32) + p4 * 8;
  unsigned ovoff[5];
#pragma unroll
  for (int rb = 0; rb < 5; ++rb) ovoff[rb] = lane * 16 + rb * 131072;

  asm volatile("" ::: ACLOB);
  sfor<20>([&](auto I) { acc_zero4<4 * decltype(I)::value>(); });
  s16x8 of[4][5];
  auto issue = [&](auto SLC, int ks) {
    constexpr int sl = decltype(SLC)::value;
    const char* gb = wog + (size_t)ks * 32 * 1024;
    dma_m0<sl * 4096>(wdst, voffw, gb); dma_m0<sl * 4096 + 1024>(wdst, voffw, gb + 8192);
    const char* ob = og + (size_t)ks * 1024;
#pragma unroll
    for (int rb = 0; rb < 5; ++rb) of[sl][rb] = gload16<0>(ovoff[rb], ob);
  };
  issue(IC<0>{}, 0); issue(IC<1>{}, 1); issue(IC<2>{}, 2);
  auto step = [&](auto PHC, int ks) {
    constexpr int PH = decltype(PHC)::value;
    if (ks < 30) WAITV(14); else WAITV(0);
    PIN5(of[PH]);
    BARRIER();
    s16x8 wf[4];
#pragma unroll
    for (int nb = 0; nb < 4; ++nb) wf[nb] = cat8(lds_tr(trb[nb] + PH * 4096), lds_tr(trb[nb] + PH * 4096 + 2048));
    SB();
    if (ks + 3 < 32) issue(IC<(PH + 3) % 4>{}, ks + 3);
    SB();
    sfor<20>([&](auto M) { constexpr int m = decltype(M)::value, nb = m / 5, rb = m % 5; mfma_acc<(rb * 4 + nb) * 4>(wf[nb], of[PH][rb]); });
  };
  for (int ks = 0; ks < 32; ks += 4) { step(IC<0>{}, ks); step(IC<1>{}, ks + 1); step(IC<2>{}, ks + 2); step(IC<3>{}, ks + 3); }
  asm volatile("s_nop 7\n\ts_nop 7" ::: "memory");
  __syncthreads();
  f32x4 mine[5];
  sfor<4>([&](auto NBc) {
    constexpr int nb = decltype(NBc)::value;
    sfor<5>([&](auto R) {
      constexpr int rb = decltype(R)::value;
      f32x4 v = {acc_rd<(rb * 4 + nb) * 4>(), acc_rd<(rb * 4 + nb) * 4 + 1>(), acc_rd<(rb * 4 + nb) * 4 + 2>(), acc_rd<(rb * 4 + nb) * 4 + 3>()};
      if (nb == kq) mine[rb] = v;
      else { const int sl = (kq - nb - 1) & 3; *(lds_f32x4_ptr)(lds0 + (((rh * 4 + nb) * 3 + sl) * 5 + rb) * 1024 + lane * 16) = v; }
    });
  });
  __syncthreads();
  char* outp = (char*)out + ((size_t)(bb & 15) * SQ + rh * 80) * 2048 + blk * 1024 + nt * 128 + kq * 32 + g * 8;
#pragma unroll
  for (int rb = 0; rb < 5; ++rb) {
    f32x4 sacc = mine[rb];
#pragma unroll
    for (int sl = 0; sl < 3; ++sl) sacc += *(lds_f32x4_ptr)(lds0 + (((rh * 4 + kq) * 3 + sl) * 5 + rb) * 1024 + lane * 16);
    uint2 o2; o2.x = cvtpk(sacc[0], sacc[1]); o2.y = cvtpk(sacc[2], sacc[3]);
    *(uint2*)(outp + (size_t)(16 * rb + li) * 2048) = o2;
  }
}

extern "C" void kernel_launch(void* const* d_in, const int* in_sizes, int n_in,
                              void* d_out, int out_size, void* d_ws, size_t ws_size,
                              hipStream_t stream) {
  const __hip_bfloat16* xA = (const __hip_bfloat16*)d_in[0];
  const __hip_bfloat16* kA = (const __hip_bfloat16*)d_in[1];
  const __hip_bfloat16* vA = (const __hip_bfloat16*)d_in[2];
  const __hip_bfloat16* wqA = (const __hip_bfloat16*)d_in[3];
  const __hip_bfloat16* woA = (const __hip_bfloat16*)d_in[4];
  const __hip_bfloat16* xB = (const __hip_bfloat16*)d_in[5];
  const __hip_bfloat16* kB = (const __hip_bfloat16*)d_in[6];
  const __hip_bfloat16* vB = (const __hip_bfloat16*)d_in[7];
  const __hip_bfloat16* wqB = (const __hip_bfloat16*)d_in[8];
  const __hip_bfloat16* woB = (const __hip_bfloat16*)d_in[9];
  __hip_bfloat16* ows = (__hip_bfloat16*)d_ws;
  attn_kernel<<<512, 512, 0, stream>>>(xA, kA, vA, wqA, xB, kB, vB, wqB, ows);
  oproj_kernel<<<256, 512, 0, stream>>>(ows, woA, woB, (__hip_bfloat16*)d_out);
}
```

```cpp
#include <hip/hip_runtime.h>
#include <hip/hip_bf16.h>
#include <utility>
#include <type_traits>

typedef __attribute__((ext_vector_type(8))) short s16x8;
typedef __attribute__((ext_vector_type(4))) short s16x4;
typedef __attribute__((ext_vector_type(4))) float f32x4;
typedef __attribute__((ext_vector_type(2))) float f32x2;
typedef __attribute__((ext_vector_type(2))) __bf16 bf16x2;
typedef __attribute__((ext_vector_type(4))) unsigned u32x4;
typedef __attribute__((address_space(3))) s16x4* lds_s16x4_ptr;
typedef __attribute__((address_space(3))) s16x8* lds_s16x8_ptr;
typedef __attribute__((address_space(3))) f32x4* lds_f32x4_ptr;

#define DI __device__ __forceinline__

constexpr int NB = 16, SQ = 160, DM = 512, NH = 32, SK = 1024, DH = 128;
constexpr int NS = 5;
constexpr int SLOT = 16384;
constexpr float QSCALE = 0.088388347648318440f * 1.4426950408889634f;
constexpr size_t WS_WO = (size_t)48 << 20;
constexpr float THR = 8.0f;

DI unsigned cvtpk(float lo, float hi) { f32x2 v = {lo, hi}; bf16x2 b = __builtin_convertvector(v, bf16x2); return __builtin_bit_cast(unsigned, b); }
DI s16x8 pack8(f32x4 a, f32x4 b) { u32x4 w = {cvtpk(a[0], a[1]), cvtpk(a[2], a[3]), cvtpk(b[0], b[1]), cvtpk(b[2], b[3])}; return __builtin_bit_cast(s16x8, w); }
DI float max3f(float a, float b, float c) { float r; asm("v_max3_f32 %0, %1, %2, %3" : "=v"(r) : "v"(a), "v"(b), "v"(c)); return r; }
DI float max2f(float a, float b) { float r; asm("v_max_f32_e32 %0, %1, %2" : "=v"(r) : "v"(a), "v"(b)); return r; }
DI f32x4 mfma16(s16x8 a, s16x8 b, f32x4 c) { return __builtin_amdgcn_mfma_f32_16x16x32_bf16(a, b, c, 0, 0, 0); }
DI s16x8 lds_b128(unsigned addr) { return *(lds_s16x8_ptr)(addr); }
DI s16x4 lds_tr(unsigned addr) { return __builtin_amdgcn_ds_read_tr16_b64_v4i16((lds_s16x4_ptr)(addr)); }
DI s16x8 cat8(s16x4 a, s16x4 b) { return __builtin_shufflevector(a, b, 0, 1, 2, 3, 4, 5, 6, 7); }

DI void dma4(unsigned ldsaddr, unsigned v0, unsigned v1, unsigned v2, unsigned v3, const void* gbase) {
  asm volatile("s_mov_b32 m0, %0\n\ts_nop 0\n\t"
               "global_load_lds_dwordx4 %1, %5 nt\n\t"
               "global_load_lds_dwordx4 %2, %5 offset:1024 nt\n\t"
               "global_load_lds_dwordx4 %3, %5 offset:2048 nt\n\t"
               "global_load_lds_dwordx4 %4, %5 offset:3072 nt"
               :: "s"(ldsaddr), "v"(v0), "v"(v1), "v"(v2), "v"(v3), "s"(gbase) : "memory");
}
DI void dma1(unsigned ldsaddr, unsigned v0, const void* gbase) {
  asm volatile("s_mov_b32 m0, %0\n\ts_nop 0\n\t"
               "global_load_lds_dwordx4 %1, %2"
               :: "s"(ldsaddr), "v"(v0), "s"(gbase) : "memory");
}
template <int OFF> DI s16x8 gload16(unsigned voff, const void* gbase) {
  s16x8 r; asm volatile("global_load_dwordx4 %0, %1, %2 offset:%3" : "=v"(r) : "v"(voff), "s"(gbase), "i"(OFF) : "memory"); return r;
}
#define WAITV(n) asm volatile("s_waitcnt vmcnt(" #n ")" ::: "memory")
#define BARRIER() asm volatile("s_barrier" ::: "memory")
#define PIN5(a) asm volatile("" : "+v"(a[0]), "+v"(a[1]), "+v"(a[2]), "+v"(a[3]), "+v"(a[4]))

#define ACLOB "a0","a1","a2","a3","a4","a5","a6","a7","a8","a9","a10","a11","a12","a13","a14","a15","a16","a17","a18","a19","a20","a21","a22","a23","a24","a25","a26","a27","a28","a29","a30","a31","a32","a33","a34","a35","a36","a37","a38","a39","a40","a41","a42","a43","a44","a45","a46","a47","a48","a49","a50","a51","a52","a53","a54","a55","a56","a57","a58","a59","a60","a61","a62","a63","a64","a65","a66","a67","a68","a69","a70","a71","a72","a73","a74","a75","a76","a77","a78","a79","a80","a81","a82","a83","a84","a85","a86","a87","a88","a89","a90","a91","a92","a93","a94","a95","a96","a97","a98","a99","a100","a101","a102","a103","a104","a105","a106","a107","a108","a109","a110","a111","a112","a113","a114","a115","a116","a117","a118","a119","a120","a121","a122","a123","a124","a125","a126","a127"
#define SB() __builtin_amdgcn_sched_barrier(0)
template <int... I, class F> DI void sfor_impl(std::integer_sequence<int, I...>, F&& f) { (f(std::integral_constant<int, I>{}), ...); }
template <int N, class F> DI void sfor(F&& f) { sfor_impl(std::make_integer_sequence<int, N>{}, f); }
template <int N> using IC = std::integral_constant<int, N>;
constexpr int AO(int rb, int db) { return (rb * 8 + db) * 4; }
constexpr int AQ(int f) { return 96 + f * 4; }
template <int A> DI void mfma_acc(s16x8 a, s16x8 b) {
  asm volatile("v_mfma_f32_16x16x32_bf16 a[%c2:%c3], %0, %1, a[%c2:%c3]" :: "v"(a), "v"(b), "i"(A), "i"(A + 3));
}
template <int A> DI void mfma_qk0(f32x4& S, s16x8 kf, const f32x4& nm) {
  asm volatile("v_mfma_f32_16x16x32_bf16 %0, %1, a[%c3:%c4], %2" : "=&v"(S) : "v"(kf), "v"(nm), "i"(A), "i"(A + 3));
}
template <int A> DI void mfma_qk(f32x4& S, s16x8 kf) {
  asm volatile("v_mfma_f32_16x16x32_bf16 %0, %1, a[%c2:%c3], %0" : "+v"(S) : "v"(kf), "i"(A), "i"(A + 3));
}
DI void mfma_v0(f32x4& D, s16x8 a, s16x8 b, const f32x4& c) { asm volatile("v_mfma_f32_16x16x32_bf16 %0, %1, %2, %3" : "=&v"(D) : "v"(a), "v"(b), "v"(c)); }
DI void mfma_v(f32x4& D, s16x8 a, s16x8 b) { asm volatile("v_mfma_f32_16x16x32_bf16 %0, %1, %2, %0" : "+v"(D) : "v"(a), "v"(b)); }
template <int A> DI float acc_rd() { float r; asm volatile("v_accvgpr_read_b32 %0, a[%c1]" : "=v"(r) : "i"(A)); return r; }
template <int A> DI void acc_wr(unsigned v) { asm volatile("v_accvgpr_write_b32 a[%c1], %0" :: "v"(v), "i"(A)); }
template <int A> DI void acc_zero4() { asm volatile("v_accvgpr_write_b32 a[%c0], 0\n\tv_accvgpr_write_b32 a[%c1], 0\n\tv_accvgpr_write_b32 a[%c2], 0\n\tv_accvgpr_write_b32 a[%c3], 0" :: "i"(A), "i"(A + 1), "i"(A + 2), "i"(A + 3)); }
template <int A> DI void acc_scale(float f) { float t; asm volatile("v_accvgpr_read_b32 %0, a[%c2]\n\tv_mul_f32 %0, %0, %1\n\tv_accvgpr_write_b32 a[%c2], %0" : "=&v"(t) : "v"(f), "i"(A)); }
DI float fadd_s(float a, float b) { float r; asm("v_add_f32_e32 %0, %1, %2" : "=v"(r) : "v"(a), "v"(b)); return r; }
template <int LOFF> DI void dma4_nt(unsigned ldsw, unsigned v0, unsigned v1, unsigned v2, unsigned v3, const void* gbase) {
  asm volatile("s_add_u32 m0, %0, %6\n\ts_nop 0\n\t"
               "global_load_lds_dwordx4 %1, %5 nt\n\t"
               "global_load_lds_dwordx4 %2, %5 offset:1024 nt\n\t"
               "global_load_lds_dwordx4 %3, %5 offset:2048 nt\n\t"
               "global_load_lds_dwordx4 %4, %5 offset:3072 nt"
               :: "s"(ldsw), "v"(v0), "v"(v1), "v"(v2), "v"(v3), "s"(gbase), "i"(LOFF) : "memory", "scc");
}
template <int LOFF> DI void dma_m0(unsigned ldsw, unsigned v0, const void* gbase) {
  asm volatile("s_add_u32 m0, %0, %3\n\ts_nop 0\n\tglobal_load_lds_dwordx4 %1, %2" :: "s"(ldsw), "v"(v0), "s"(gbase), "i"(LOFF) : "memory", "scc");
}
#define TS(v) do { } while (0)
constexpr int TILE = 8192;
constexpr int XREG = 120 * 1024, XCH = 20480;

template <int NRB>
DI void attn_body(const unsigned short* wsrc, char* wdst, const char* xg, const char* wqg, const char* kg, const char* vg, char* ob, const unsigned lds0, const unsigned ldsh, const int r, const int bw, const int row0, const int lane
                  ) {
  constexpr bool SMALL = NRB == 2;
  const int li = lane & 15, g = lane >> 4, q4 = (lane >> 2) & 3, p4 = lane & 3;
  const unsigned ldsw = ldsh + r * 4096;
  unsigned voffK[4], voffV[2], voffW[2];
#pragma unroll
  for (int n = 0; n < 4; ++n) voffK[n] = g * 256 + (((li ^ g) ^ (4 * n)) * 16);
#pragma unroll
  for (int n = 0; n < 2; ++n) voffV[n] = g * 256 + ((li ^ (g << 1) ^ (n << 3)) * 16);
#pragma unroll
  for (int n = 0; n < 2; ++n) voffW[n] = g * 8192 + ((li ^ ((g | (n << 2)) << 1)) * 16);
  unsigned kofs[4], wofs[4], vofs[8];
#pragma unroll
  for (int ks = 0; ks < 4; ++ks) kofs[ks] = ldsh + li * 256 + (((4 * ks + g) ^ li) * 16);
  const unsigned r7 = 4 * (g & 1) + q4;
#pragma unroll
  for (int db = 0; db < 8; ++db) vofs[db] = ldsh + (4 * g + q4) * 256 + (r7 ^ db) * 32 + (p4 >> 1) * 16 + (p4 & 1) * 8;
#pragma unroll
  for (int t = 0; t < 4; ++t) wofs[t] = ldsh + 4 * TILE + (8 * g + q4) * 256 + (((4 * t + p4) ^ (2 * q4 | ((g & 1) << 3))) * 16);
  unsigned voffX[2], xofs[2];
#pragma unroll
  for (int n = 0; n < 2; ++n) voffX[n] = (lane >> 3) * 1024 + (((lane & 7) ^ (4 * ((n + bw) & 1) + g)) * 16);
#pragma unroll
  for (int kk = 0; kk < 2; ++kk) xofs[kk] = lds0 + XREG + (row0 + li) * 128 + (((4 * kk + g) ^ (li >> 1)) * 16);

  unsigned long long ts0 = 0, ts1 = 0, ts3 = 0, ts4 = 0, tw0 = 0, tw1 = 0, twsum = 0, tqw = 0;
  (void)ts0; (void)ts1; (void)ts3; (void)ts4; (void)tw0; (void)tw1; (void)twsum; (void)tqw;
  TS(ts0);

  auto tileW = [&](auto N) { constexpr int n = decltype(N)::value;
    sfor<4>([&](auto PC) { constexpr int pc = decltype(PC)::value;
      dma_m0<(4 + n % 3) * TILE + pc * 1024>(ldsw, voffW[(pc >> 1) & 1], wqg + (size_t)(32 * n) * 8192 + (size_t)pc * 32768); }); };
  auto tileK = [&](auto SL, const char* gb) { dma4_nt<decltype(SL)::value * TILE>(ldsw, voffK[0], voffK[1], voffK[2], voffK[3], gb); };
  auto tileV = [&](auto SL, const char* gb) { dma4_nt<decltype(SL)::value * TILE>(ldsw, voffV[0], voffV[1], voffV[0], voffV[1], gb); };

  const unsigned ldsx = lds0 + XREG + bw * 5120;
  const char* xgw = xg + (size_t)bw * 40 * 1024;
  auto chunkX = [&](auto C) { constexpr int c = decltype(C)::value;
    sfor<5>([&](auto PC) { constexpr int pc = decltype(PC)::value;
      dma_m0<(c & 1) * XCH + pc * 1024>(ldsx, voffX[pc & 1], xgw + (size_t)pc * 8192 + c * 128); }); };
  if constexpr (SMALL) { tileW(IC<0>{}); tileW(IC<1>{}); tileW(IC<2>{}); } else { chunkX(IC<0>{}); chunkX(IC<1>{}); }
  {
#pragma unroll
    for (int f = 0; f < 2; ++f) {
      s16x8 v;
#pragma unroll
      for (int j = 0; j < 8; ++j) v[j] = (short)wsrc[(size_t)((j < 4 ? 4 * g + j : 12 + 4 * g + j)) * DM + f * 16 + li];
      *(s16x8*)(wdst + f * 1024 + lane * 16) = v;
    }
  }
  asm volatile("" ::: ACLOB);
  sfor<NRB * 8>([&](auto I) { acc_zero4<4 * decltype(I)::value>(); });
  s16x8 wf[2][8], xf[2][NRB];
  auto wread = [&](auto N, auto I) { constexpr int n = decltype(N)::value, i = decltype(I)::value;
    const unsigned a = wofs[i >> 1] + (n % 3) * TILE + (i & 1) * 8; wf[n & 1][i] = cat8(lds_tr(a), lds_tr(a + 1024)); };
  auto xread = [&](auto N, auto R) { constexpr int n = decltype(N)::value, rb = decltype(R)::value;
    xf[n & 1][rb] = lds_b128(xofs[n & 1] + ((n >> 1) & 1) * XCH + rb * 2048); };
  if constexpr (SMALL) WAITV(8); else WAITV(5);
  BARRIER();
  sfor<NRB>([&](auto R) { xread(IC<0>{}, R); });
  sfor<8>([&](auto I) { wread(IC<0>{}, I); });
  auto qstep = [&](auto N, auto WSC) {
    constexpr int n = decltype(N)::value;
    constexpr int WN = SMALL ? decltype(WSC)::value : ((n & 1) ? 0 : 63);
    TS(tw0);
    asm volatile("s_waitcnt vmcnt(%0) lgkmcnt(0)" :: "i"(WN) : "memory");
    BARRIER();
    TS(tw1); tqw += tw1 - tw0;
    SB();
    if constexpr (SMALL) {
      if constexpr (n + 3 < 16) tileW(IC<n + 3>{});
      if constexpr (n < 4) { if constexpr ((n & 1) == 0) tileK(IC<n>{}, kg + (size_t)(n / 2) * 8192); else tileV(IC<n>{}, vg + (size_t)(n / 2) * 8192); }
      if constexpr (n == 13) tileV(IC<5>{}, vg + (size_t)2 * 8192);
    } else {
      if constexpr ((n & 1) == 1 && (n + 3) / 2 < 8) chunkX(IC<(n + 3) / 2>{});
    }
    SB();
    sfor<8 * NRB>([&](auto M) { constexpr int m = decltype(M)::value, d = m / NRB, rb = m % NRB;
      mfma_acc<AO(rb, d)>(wf[n & 1][d], xf[n & 1][rb]);
      if constexpr (n < 15) {
        if constexpr (m < NRB) xread(IC<n + 1>{}, IC<m>{});
        else if constexpr (m < NRB + 8) wread(IC<n + 1>{}, IC<m - NRB>{});
      }
      SB();
    });
  };
  qstep(IC<0>{}, IC<4>{}); qstep(IC<1>{}, IC<8>{}); qstep(IC<2>{}, IC<12>{}); qstep(IC<3>{}, IC<12>{});
  qstep(IC<4>{}, IC<12>{}); qstep(IC<5>{}, IC<8>{}); qstep(IC<6>{}, IC<4>{}); qstep(IC<7>{}, IC<4>{});
  qstep(IC<8>{}, IC<4>{}); qstep(IC<9>{}, IC<4>{}); qstep(IC<10>{}, IC<4>{}); qstep(IC<11>{}, IC<4>{});
  qstep(IC<12>{}, IC<4>{}); qstep(IC<13>{}, IC<4>{}); qstep(IC<14>{}, IC<4>{}); qstep(IC<15>{}, IC<4>{});
  if constexpr (SMALL) tileK(IC<4>{}, kg + (size_t)2 * 8192);
  asm volatile("s_nop 7\n\ts_nop 7" ::: "memory");
  s16x8 Qv[4];
  sfor<NRB>([&](auto R) { sfor<4>([&](auto Tt) {
    constexpr int rb = decltype(R)::value, t = decltype(Tt)::value, f = rb * 4 + t;
    float v[8];
    sfor<8>([&](auto E) { v[decltype(E)::value] = acc_rd<AO(rb, 2 * t) + decltype(E)::value>() * QSCALE; });
    if constexpr (f < 8) sfor<4>([&](auto E) { constexpr int e = decltype(E)::value; acc_wr<AQ(f) + e>(cvtpk(v[2 * e], v[2 * e + 1])); });
    else { u32x4 q = {cvtpk(v[0], v[1]), cvtpk(v[2], v[3]), cvtpk(v[4], v[5]), cvtpk(v[6], v[7])}; Qv[f - 8] = __builtin_bit_cast(s16x8, q); }
  }); });
  sfor<NRB * 8>([&](auto I) { acc_zero4<4 * decltype(I)::value>(); });

  f32x4 NM[NRB], S[NRB][2];
  float lsum[NRB];
  unsigned Pw[NRB][4];
#pragma unroll
  for (int rb = 0; rb < NRB; ++rb) { lsum[rb] = 0.f; NM[rb] = f32x4{0.f, 0.f, 0.f, 0.f}; }
  auto rowmax = [&](const f32x4 (&Sr)[2]) {
    float t = max3f(Sr[0][0], Sr[0][1], Sr[0][2]);
    t = max3f(t, Sr[0][3], Sr[1][0]); t = max3f(t, Sr[1][1], Sr[1][2]); t = max2f(t, Sr[1][3]);
    return t;
  };
  auto xlane_max = [&](float t) { t = fmaxf(t, __shfl_xor(t, 16)); t = fmaxf(t, __shfl_xor(t, 32)); return t; };
  auto sm_item = [&](int rb, int k) {
    if (k < 8) { float t = __builtin_amdgcn_exp2f(S[rb][k >> 2][k & 3]); asm volatile("" : "+v"(t)); S[rb][k >> 2][k & 3] = t; }
    else if (k < 16) { float t = fadd_s(lsum[rb], S[rb][(k - 8) >> 2][(k - 8) & 3]); asm volatile("" : "+v"(t)); lsum[rb] = t; }
    else { const int c = k - 16; unsigned t = cvtpk(S[rb][c >> 1][(c & 1) * 2], S[rb][c >> 1][(c & 1) * 2 + 1]); asm volatile("" : "+v"(t)); Pw[rb][c] = t; }
  };
  auto Pf = [&](int rb) { u32x4 v = {Pw[rb][0], Pw[rb][1], Pw[rb][2], Pw[rb][3]}; return __builtin_bit_cast(s16x8, v); };

  TS(ts1);
  auto aiter = [&](int j, auto PHC) {
    constexpr int PH = decltype(PHC)::value;
    constexpr int KS = 2 * PH, VS = 2 * PH + 1, KD = 2 * ((PH + 3) % 4), VD = KD + 1;
    TS(tw0);
    if constexpr (SMALL) { if (j < 30) WAITV(16); else WAITV(0); }
    BARRIER();
    TS(tw1); twsum += tw1 - tw0;
    s16x8 kf[2][4], vf[8];
#pragma unroll
    for (int m = 0; m < 8; ++m) kf[m >> 2][m & 3] = lds_b128(kofs[m & 3] + KS * TILE + (m >> 2) * 4096);
    SB();
    if constexpr (SMALL) { if (j + 3 < 32) { tileK(IC<KD>{}, kg + (size_t)(j + 3) * 8192); tileV(IC<VD>{}, vg + (size_t)(j + 3) * 8192); } }
    SB();
    sfor<8 * NRB>([&](auto M) {
      constexpr int m = decltype(M)::value, g8 = m / NRB, rb = m % NRB, kb = g8 >> 2, ks = g8 & 3, f = rb * 4 + ks;
      if constexpr (f < 8) { if constexpr (ks == 0) mfma_qk0<AQ(f)>(S[rb][kb], kf[kb][ks], NM[rb]); else mfma_qk<AQ(f)>(S[rb][kb], kf[kb][ks]); }
      else { if constexpr (ks == 0) mfma_v0(S[rb][kb], kf[kb][ks], Qv[f - 8], NM[rb]); else mfma_v(S[rb][kb], kf[kb][ks], Qv[f - 8]); }
      if constexpr (rb == NRB - 1) { const unsigned a = vofs[g8] + VS * TILE; vf[g8] = cat8(lds_tr(a), lds_tr(a + 4096)); }
      SB();
    });
    if constexpr (NRB == 3) asm volatile("s_nop 7" : "+v"(S[0][0]), "+v"(S[0][1]), "+v"(S[1][0]), "+v"(S[1][1]), "+v"(S[2][0]), "+v"(S[2][1]));
    else asm volatile("s_nop 7" : "+v"(S[0][0]), "+v"(S[0][1]), "+v"(S[1][0]), "+v"(S[1][1]));
    {
      float ta = max2f(S[0][0][0], S[0][0][1]), tb = max2f(S[0][0][2], S[0][0][3]);
#pragma unroll
      for (int i = 1; i < 2 * NRB; ++i) { ta = max3f(ta, S[i >> 1][i & 1][0], S[i >> 1][i & 1][1]); tb = max3f(tb, S[i >> 1][i & 1][2], S[i >> 1][i & 1][3]); }
      const bool first = j == 0;
      if (__builtin_expect(first || __any(max2f(ta, tb) > THR), 0)) {
        sfor<NRB>([&](auto R) {
          constexpr int rb = decltype(R)::value;
          const float rm = xlane_max(rowmax(S[rb]));
          const float dl = first ? rm : fmaxf(rm, 0.f);
          const float al = first ? 1.f : __builtin_amdgcn_exp2f(-dl);
          S[rb][0] -= dl; S[rb][1] -= dl; NM[rb] -= dl; lsum[rb] *= al;
          sfor<32>([&](auto E) { acc_scale<AO(rb, 0) + decltype(E)::value>(al); });
        });
      }
    }
#pragma unroll
    for (int k = 0; k < 20; ++k) sm_item(0, k);
    asm volatile("s_nop 1" ::: "memory");
    SB();
    sfor<8 * NRB>([&](auto M) {
      constexpr int m = decltype(M)::value, rb = m / 8, db = m % 8;
      mfma_acc<AO(rb, db)>(vf[db], Pf(rb));
      if constexpr (rb + 1 < NRB) {
        constexpr int k0 = db < 4 ? 2 * db : 8 + 3 * (db - 4), k1 = db < 4 ? 2 * db + 2 : 8 + 3 * (db - 3);
#pragma unroll
        for (int k = k0; k < k1; ++k) sm_item(rb + 1, k);
        if constexpr (db == 7) asm volatile("s_nop 1" ::: "memory");
      }
      SB();
    });
  };
  for (int j = 0; j < 32; j += 4) { aiter(j, IC<0>{}); aiter(j + 1, IC<1>{}); aiter(j + 2, IC<2>{}); aiter(j + 3, IC<3>{}); }

  TS(ts3);
  asm volatile("s_nop 7\n\ts_nop 7" ::: "memory");
  sfor<NRB>([&](auto R) {
    constexpr int rb = decltype(R)::value;
    float l = lsum[rb]; l += __shfl_xor(l, 16); l += __shfl_xor(l, 32);
    const float il = __builtin_amdgcn_rcpf(l);
    sfor<4>([&](auto Tt) {
      constexpr int t = decltype(Tt)::value;
      float v[8];
      sfor<8>([&](auto E) { v[decltype(E)::value] = acc_rd<AO(rb, 2 * t) + decltype(E)::value>() * il; });
      u32x4 o = {cvtpk(v[0], v[1]), cvtpk(v[2], v[3]), cvtpk(v[4], v[5]), cvtpk(v[6], v[7])};
      *(u32x4*)(ob + (size_t)rb * (NH * 4 * 1024) + t * 1024) = o;
    });
  });
}

__global__ __launch_bounds__(512, 2)
void attn_kernel(const __hip_bfloat16* __restrict__ xA, const __hip_bfloat16* __restrict__ kA, const __hip_bfloat16* __restrict__ vA, const __hip_bfloat16* __restrict__ wqA,
                 const __hip_bfloat16* __restrict__ xB, const __hip_bfloat16* __restrict__ kB, const __hip_bfloat16* __restrict__ vB, const __hip_bfloat16* __restrict__ wqB,
                 const __hip_bfloat16* __restrict__ woA, const __hip_bfloat16* __restrict__ woB, __hip_bfloat16* __restrict__ ows) {
  __shared__ __attribute__((aligned(16))) char smem[20 * TILE];
  const unsigned lds0 = (unsigned)(size_t)smem;
  const int tid = threadIdx.x, lane = tid & 63;
  const int w = __builtin_amdgcn_readfirstlane(tid >> 6);
  const int e = w & 1, r = (w >> 1) & 1;
  const int idx = blockIdx.x, xcd = idx & 7, j0 = idx >> 3, blk = j0 >> 5, jj = j0 & 31;
  const int b = (xcd >> 2) * 8 + (jj >> 2), h = ((xcd & 3) * 4 + (jj & 3)) * 2 + e;
  const char* xg = (const char*)(blk ? xB : xA) + (size_t)b * SQ * DM * 2;
  const char* wqg = (const char*)(blk ? wqB : wqA) + (size_t)h * DH * 2 + (size_t)r * 16 * 8192;
  const char* kg = (const char*)(blk ? kB : kA) + ((size_t)b * NH + h) * SK * DH * 2 + r * 4096;
  const char* vg = (const char*)(blk ? vB : vA) + ((size_t)b * NH + h) * SK * DH * 2 + r * 4096;
  const unsigned ldsh = lds0 + e * (8 * TILE);
  const int rbg = w < 4 ? 3 * r : 6 + 2 * r;
  char* ob = (char*)ows + ((((size_t)(blk * NB + b) * 10 + rbg) * NH + h) * 4) * 1024 + lane * 16;
  const int rp = idx & 255, rnt = rp & 7, rks = (rp >> 3) * 4 + (w >> 1);
  const unsigned short* wsrc = (const unsigned short*)((idx >> 8) ? woB : woA) + (size_t)rks * 32 * DM + rnt * 64 + (w & 1) * 32;
  char* wdst = (char*)ows + WS_WO + ((size_t)((idx >> 8) * 8 + rnt) * 128 + rks) * 4096 + (w & 1) * 2048;
  if (w < 4) attn_body<3>(wsrc, wdst, xg, wqg, kg, vg, ob, lds0, ldsh, r, w & 3, 16 * rbg, lane); else attn_body<2>(wsrc, wdst, xg, wqg, kg, vg, ob, lds0, ldsh, r, w & 3, 16 * rbg, lane);
}

constexpr int K2_LDS = 8 * 15 * 1024;
__global__ __launch_bounds__(512, 2)
void oproj_kernel(const char* __restrict__ ws, __hip_bfloat16* __restrict__ out) {
  __shared__ __attribute__((aligned(16))) char smem[K2_LDS];
  const unsigned lds0 = (unsigned)(size_t)smem;
  const int tid = threadIdx.x, lane = tid & 63;
  const int w = __builtin_amdgcn_readfirstlane(tid >> 6);
  const int rh = w >> 2, kq = w & 3;
  const int li = lane & 15, g = lane >> 4;
  const int idx = blockIdx.x, xcd = idx & 7, r = idx >> 3;
  const int nt = r & 7, bb = (r >> 3) * 8 + xcd;
  const int blk = bb >> 4;
  const char* og = ws + ((size_t)bb * 10 + rh * 5) * 128 * 1024 + (size_t)kq * 32 * 1024;
  const char* wg = ws + WS_WO + ((size_t)(blk * 8 + nt) * 128 + kq * 32) * 4096;
  unsigned ovoff[5];
#pragma unroll
  for (int rb = 0; rb < 5; ++rb) ovoff[rb] = lane * 16 + rb * 131072;

  asm volatile("" ::: ACLOB);
  s16x8 of[3][5], wf[3][4];
  auto ld = [&](auto SLC, auto IT, int ks) {
    constexpr int sl = decltype(SLC)::value, it = decltype(IT)::value;
    if constexpr (it < 4) wf[sl][it] = gload16<it * 1024>(ovoff[0], wg + (size_t)ks * 4096);
    else of[sl][it - 4] = gload16<0>(ovoff[it - 4], og + (size_t)ks * 1024);
  };
  sfor<9>([&](auto I) { ld(IC<0>{}, I, 0); });
  sfor<9>([&](auto I) { ld(IC<1>{}, I, 1); });
  sfor<20>([&](auto I) { acc_zero4<4 * decltype(I)::value>(); });
  auto step = [&](auto PHC, int ks) {
    constexpr int PH = decltype(PHC)::value;
    const int kn = ks + 2 < 32 ? ks + 2 : 31;
    WAITV(9);
    PIN5(of[PH]); asm volatile("" : "+v"(wf[PH][0]), "+v"(wf[PH][1]), "+v"(wf[PH][2]), "+v"(wf[PH][3]));
    SB();
    sfor<20>([&](auto M) { constexpr int m = decltype(M)::value, nb = m / 5, rb = m % 5;
      mfma_acc<(rb * 4 + nb) * 4>(wf[PH][nb], of[PH][rb]);
      if constexpr ((m & 1) == 0 && m / 2 < 9) ld(IC<(PH + 2) % 3>{}, IC<m / 2>{}, kn);
      SB();
    });
  };
  for (int ks = 0; ks < 30; ks += 3) { step(IC<0>{}, ks); step(IC<1>{}, ks + 1); step(IC<2>{}, ks + 2); }
  step(IC<0>{}, 30); step(IC<1>{}, 31);
  asm volatile("s_waitcnt vmcnt(0)\n\ts_nop 7\n\ts_nop 7" ::: "memory");
  f32x4 mine[5];
  sfor<4>([&](auto NBc) {
    constexpr int nb = decltype(NBc)::value;
    sfor<5>([&](auto R) {
      constexpr int rb = decltype(R)::value;
      f32x4 v = {acc_rd<(rb * 4 + nb) * 4>(), acc_rd<(rb * 4 + nb) * 4 + 1>(), acc_rd<(rb * 4 + nb) * 4 + 2>(), acc_rd<(rb * 4 + nb) * 4 + 3>()};
      if (nb == kq) mine[rb] = v;
      else { const int sl = (kq - nb - 1) & 3; *(lds_f32x4_ptr)(lds0 + (((rh * 4 + nb) * 3 + sl) * 5 + rb) * 1024 + lane * 16) = v; }
    });
  });
  __syncthreads();
  char* outp = (char*)out + ((size_t)(bb & 15) * SQ + rh * 80) * 2048 + blk * 1024 + nt * 128 + kq * 32 + g * 8;
#pragma unroll
  for (int rb = 0; rb < 5; ++rb) {
    f32x4 sacc = mine[rb];
#pragma unroll
    for (int sl = 0; sl < 3; ++sl) sacc += *(lds_f32x4_ptr)(lds0 + (((rh * 4 + kq) * 3 + sl) * 5 + rb) * 1024 + lane * 16);
    uint2 o2; o2.x = cvtpk(sacc[0], sacc[1]); o2.y = cvtpk(sacc[2], sacc[3]);
    *(uint2*)(outp + (size_t)(16 * rb + li) * 2048) = o2;
  }
}

extern "C" void kernel_launch(void* const* d_in, const int* in_sizes, int n_in,
                              void* d_out, int out_size, void* d_ws, size_t ws_size,
                              hipStream_t stream) {
  const __hip_bfloat16* xA = (const __hip_bfloat16*)d_in[0];
  const __hip_bfloat16* kA = (const __hip_bfloat16*)d_in[1];
  const __hip_bfloat16* vA = (const __hip_bfloat16*)d_in[2];
  const __hip_bfloat16* wqA = (const __hip_bfloat16*)d_in[3];
  const __hip_bfloat16* woA = (const __hip_bfloat16*)d_in[4];
  const __hip_bfloat16* xB = (const __hip_bfloat16*)d_in[5];
  const __hip_bfloat16* kB = (const __hip_bfloat16*)d_in[6];
  const __hip_bfloat16* vB = (const __hip_bfloat16*)d_in[7];
  const __hip_bfloat16* wqB = (const __hip_bfloat16*)d_in[8];
  const __hip_bfloat16* woB = (const __hip_bfloat16*)d_in[9];
  __hip_bfloat16* ows = (__hip_bfloat16*)d_ws;
  attn_kernel<<<512, 512, 0, stream>>>(xA, kA, vA, wqA, xB, kB, vB, wqB, woA, woB, ows);
  oproj_kernel<<<256, 512, 0, stream>>>((const char*)d_ws, (__hip_bfloat16*)d_out);
}
```

```cpp
#include <hip/hip_runtime.h>
#include <hip/hip_bf16.h>
#include <utility>
#include <type_traits>

typedef __attribute__((ext_vector_type(8))) short s16x8;
typedef __attribute__((ext_vector_type(4))) short s16x4;
typedef __attribute__((ext_vector_type(4))) float f32x4;
typedef __attribute__((ext_vector_type(2))) float f32x2;
typedef __attribute__((ext_vector_type(2))) __bf16 bf16x2;
typedef __attribute__((ext_vector_type(4))) unsigned u32x4;
typedef __attribute__((address_space(3))) s16x4* lds_s16x4_ptr;
typedef __attribute__((address_space(3))) s16x8* lds_s16x8_ptr;
typedef __attribute__((address_space(3))) f32x4* lds_f32x4_ptr;

#define DI __device__ __forceinline__

constexpr int NB = 16, SQ = 160, DM = 512, NH = 32, SK = 1024, DH = 128;
constexpr int NS = 5;
constexpr int SLOT = 16384;
constexpr float QSCALE = 0.088388347648318440f * 1.4426950408889634f;
constexpr size_t WS_WO = (size_t)48 << 20;
constexpr float THR = 8.0f;

DI unsigned cvtpk(float lo, float hi) { f32x2 v = {lo, hi}; bf16x2 b = __builtin_convertvector(v, bf16x2); return __builtin_bit_cast(unsigned, b); }
DI s16x8 pack8(f32x4 a, f32x4 b) { u32x4 w = {cvtpk(a[0], a[1]), cvtpk(a[2], a[3]), cvtpk(b[0], b[1]), cvtpk(b[2], b[3])}; return __builtin_bit_cast(s16x8, w); }
DI float max3f(float a, float b, float c) { float r; asm("v_max3_f32 %0, %1, %2, %3" : "=v"(r) : "v"(a), "v"(b), "v"(c)); return r; }
DI float max2f(float a, float b) { float r; asm("v_max_f32_e32 %0, %1, %2" : "=v"(r) : "v"(a), "v"(b)); return r; }
DI f32x4 mfma16(s16x8 a, s16x8 b, f32x4 c) { return __builtin_amdgcn_mfma_f32_16x16x32_bf16(a, b, c, 0, 0, 0); }
DI s16x8 lds_b128(unsigned addr) { return *(lds_s16x8_ptr)(addr); }
DI s16x4 lds_tr(unsigned addr) { return __builtin_amdgcn_ds_read_tr16_b64_v4i16((lds_s16x4_ptr)(addr)); }
DI s16x8 cat8(s16x4 a, s16x4 b) { return __builtin_shufflevector(a, b, 0, 1, 2, 3, 4, 5, 6, 7); }

DI void dma4(unsigned ldsaddr, unsigned v0, unsigned v1, unsigned v2, unsigned v3, const void* gbase) {
  asm volatile("s_mov_b32 m0, %0\n\ts_nop 0\n\t"
               "global_load_lds_dwordx4 %1, %5 nt\n\t"
               "global_load_lds_dwordx4 %2, %5 offset:1024 nt\n\t"
               "global_load_lds_dwordx4 %3, %5 offset:2048 nt\n\t"
               "global_load_lds_dwordx4 %4, %5 offset:3072 nt"
               :: "s"(ldsaddr), "v"(v0), "v"(v1), "v"(v2), "v"(v3), "s"(gbase) : "memory");
}
DI void dma1(unsigned ldsaddr, unsigned v0, const void* gbase) {
  asm volatile("s_mov_b32 m0, %0\n\ts_nop 0\n\t"
               "global_load_lds_dwordx4 %1, %2"
               :: "s"(ldsaddr), "v"(v0), "s"(gbase) : "memory");
}
template <int OFF> DI s16x8 gload16(unsigned voff, const void* gbase) {
  s16x8 r; asm volatile("global_load_dwordx4 %0, %1, %2 offset:%3" : "=v"(r) : "v"(voff), "s"(gbase), "i"(OFF) : "memory"); return r;
}
#define WAITV(n) asm volatile("s_waitcnt vmcnt(" #n ")" ::: "memory")
#define BARRIER() asm volatile("s_barrier" ::: "memory")
#define PIN5(a) asm volatile("" : "+v"(a[0]), "+v"(a[1]), "+v"(a[2]), "+v"(a[3]), "+v"(a[4]))

#define ACLOB "a0","a1","a2","a3","a4","a5","a6","a7","a8","a9","a10","a11","a12","a13","a14","a15","a16","a17","a18","a19","a20","a21","a22","a23","a24","a25","a26","a27","a28","a29","a30","a31","a32","a33","a34","a35","a36","a37","a38","a39","a40","a41","a42","a43","a44","a45","a46","a47","a48","a49","a50","a51","a52","a53","a54","a55","a56","a57","a58","a59","a60","a61","a62","a63","a64","a65","a66","a67","a68","a69","a70","a71","a72","a73","a74","a75","a76","a77","a78","a79","a80","a81","a82","a83","a84","a85","a86","a87","a88","a89","a90","a91","a92","a93","a94","a95","a96","a97","a98","a99","a100","a101","a102","a103","a104","a105","a106","a107","a108","a109","a110","a111","a112","a113","a114","a115","a116","a117","a118","a119","a120","a121","a122","a123","a124","a125","a126","a127"
#define SB() __builtin_amdgcn_sched_barrier(0)
template <int... I, class F> DI void sfor_impl(std::integer_sequence<int, I...>, F&& f) { (f(std::integral_constant<int, I>{}), ...); }
template <int N, class F> DI void sfor(F&& f) { sfor_impl(std::make_integer_sequence<int, N>{}, f); }
template <int N> using IC = std::integral_constant<int, N>;
constexpr int AO(int rb, int db) { return (rb * 8 + db) * 4; }
constexpr int AQ(int f) { return 96 + f * 4; }
template <int A> DI void mfma_acc(s16x8 a, s16x8 b) {
  asm volatile("v_mfma_f32_16x16x32_bf16 a[%c2:%c3], %0, %1, a[%c2:%c3]" :: "v"(a), "v"(b), "i"(A), "i"(A + 3));
}
template <int A> DI void mfma_qk0(f32x4& S, s16x8 kf, const f32x4& nm) {
  asm volatile("v_mfma_f32_16x16x32_bf16 %0, %1, a[%c3:%c4], %2" : "=&v"(S) : "v"(kf), "v"(nm), "i"(A), "i"(A + 3));
}
template <int A> DI void mfma_qk(f32x4& S, s16x8 kf) {
  asm volatile("v_mfma_f32_16x16x32_bf16 %0, %1, a[%c2:%c3], %0" : "+v"(S) : "v"(kf), "i"(A), "i"(A + 3));
}
DI void mfma_v0(f32x4& D, s16x8 a, s16x8 b, const f32x4& c) { asm volatile("v_mfma_f32_16x16x32_bf16 %0, %1, %2, %3" : "=&v"(D) : "v"(a), "v"(b), "v"(c)); }
DI void mfma_v(f32x4& D, s16x8 a, s16x8 b) { asm volatile("v_mfma_f32_16x16x32_bf16 %0, %1, %2, %0" : "+v"(D) : "v"(a), "v"(b)); }
template <int A> DI float acc_rd() { float r; asm volatile("v_accvgpr_read_b32 %0, a[%c1]" : "=v"(r) : "i"(A)); return r; }
template <int A> DI void acc_wr(unsigned v) { asm volatile("v_accvgpr_write_b32 a[%c1], %0" :: "v"(v), "i"(A)); }
template <int A> DI void acc_zero4() { asm volatile("v_accvgpr_write_b32 a[%c0], 0\n\tv_accvgpr_write_b32 a[%c1], 0\n\tv_accvgpr_write_b32 a[%c2], 0\n\tv_accvgpr_write_b32 a[%c3], 0" :: "i"(A), "i"(A + 1), "i"(A + 2), "i"(A + 3)); }
template <int A> DI void acc_scale(float f) { float t; asm volatile("v_accvgpr_read_b32 %0, a[%c2]\n\tv_mul_f32 %0, %0, %1\n\tv_accvgpr_write_b32 a[%c2], %0" : "=&v"(t) : "v"(f), "i"(A)); }
DI float fadd_s(float a, float b) { float r; asm("v_add_f32_e32 %0, %1, %2" : "=v"(r) : "v"(a), "v"(b)); return r; }
template <int LOFF> DI void dma4_nt(unsigned ldsw, unsigned v0, unsigned v1, unsigned v2, unsigned v3, const void* gbase) {
  asm volatile("s_add_u32 m0, %0, %6\n\ts_nop 0\n\t"
               "global_load_lds_dwordx4 %1, %5 nt\n\t"
               "global_load_lds_dwordx4 %2, %5 offset:1024 nt\n\t"
               "global_load_lds_dwordx4 %3, %5 offset:2048 nt\n\t"
               "global_load_lds_dwordx4 %4, %5 offset:3072 nt"
               :: "s"(ldsw), "v"(v0), "v"(v1), "v"(v2), "v"(v3), "s"(gbase), "i"(LOFF) : "memory", "scc");
}
template <int LOFF> DI void dma_m0(unsigned ldsw, unsigned v0, const void* gbase) {
  asm volatile("s_add_u32 m0, %0, %3\n\ts_nop 0\n\tglobal_load_lds_dwordx4 %1, %2" :: "s"(ldsw), "v"(v0), "s"(gbase), "i"(LOFF) : "memory", "scc");
}
#define TS(v) do { } while (0)
constexpr int TILE = 8192;
constexpr int XREG = 120 * 1024, XCH = 20480;

template <int NRB>
DI void attn_body(const unsigned short* wsrc, char* wdst, const char* xg, const char* wqg, const char* kg, const char* vg, char* ob, const unsigned lds0, const unsigned ldsh, const int r, const int bw, const int row0, const int lane
                  ) {
  constexpr bool SMALL = NRB == 2;
  const int li = lane & 15, g = lane >> 4, q4 = (lane >> 2) & 3, p4 = lane & 3;
  const unsigned ldsw = ldsh + r * 4096;
  unsigned voffK[4], voffV[2];
  const int hw = SMALL ? 2 + r : r;
#pragma unroll
  for (int n = 0; n < 4; ++n) voffK[n] = g * 256 + (((li ^ g) ^ (4 * n)) * 16);
#pragma unroll
  for (int n = 0; n < 2; ++n) voffV[n] = g * 256 + ((li ^ (g << 1) ^ (n << 3)) * 16);
  const unsigned voffW = g * 8192 + ((li ^ ((g & 1) | ((g >> 1) << 2) | (r << 3))) * 16);
  const unsigned ldsq = ldsh + hw * 2048;
  const char* wqw = wqg + (size_t)hw * 8 * 8192;
  unsigned kofs[4], wofs[4], vofs[8];
#pragma unroll
  for (int ks = 0; ks < 4; ++ks) kofs[ks] = ldsh + li * 256 + (((4 * ks + g) ^ li) * 16);
  const unsigned r7 = 4 * (g & 1) + q4;
#pragma unroll
  for (int db = 0; db < 8; ++db) vofs[db] = ldsh + (4 * g + q4) * 256 + (r7 ^ db) * 32 + (p4 >> 1) * 16 + (p4 & 1) * 8;
#pragma unroll
  for (int t = 0; t < 4; ++t) wofs[t] = ldsh + 4 * TILE + (8 * g + q4) * 256 + (((4 * t + p4) ^ ((q4 & 1) | ((q4 >> 1) << 2) | ((g & 1) << 3))) * 16) + (p4 & 1) * 8;
  unsigned voffX[2], xofs[2];
#pragma unroll
  for (int n = 0; n < 2; ++n) voffX[n] = (lane >> 3) * 1024 + (((lane & 7) ^ (4 * ((n + bw) & 1) + g)) * 16);
#pragma unroll
  for (int kk = 0; kk < 2; ++kk) xofs[kk] = lds0 + XREG + (row0 + li) * 128 + (((4 * kk + g) ^ (li >> 1)) * 16);

  unsigned long long tqa = 0, tqb = 0; (void)tqa; (void)tqb;
  unsigned long long ts0 = 0, ts1 = 0, ts3 = 0, ts4 = 0, tw0 = 0, tw1 = 0, twsum = 0, tqw = 0;
  (void)ts0; (void)ts1; (void)ts3; (void)ts4; (void)tw0; (void)tw1; (void)twsum; (void)tqw;
  TS(ts0);

  auto tileW = [&](auto N) { constexpr int n = decltype(N)::value;
    dma_m0<(4 + n % 3) * TILE>(ldsq, voffW, wqw + (size_t)(32 * n) * 8192); dma_m0<(4 + n % 3) * TILE + 1024>(ldsq, voffW, wqw + (size_t)(32 * n) * 8192 + 32768); };
  auto tileK = [&](auto SL, const char* gb) { dma4_nt<decltype(SL)::value * TILE>(ldsw, voffK[0], voffK[1], voffK[2], voffK[3], gb); };
  auto tileV = [&](auto SL, const char* gb) { dma4_nt<decltype(SL)::value * TILE>(ldsw, voffV[0], voffV[1], voffV[0], voffV[1], gb); };

  const unsigned ldsx = lds0 + XREG + bw * 5120;
  const char* xgw = xg + (size_t)bw * 40 * 1024;
  auto chunkX = [&](auto C) { constexpr int c = decltype(C)::value;
    sfor<5>([&](auto PC) { constexpr int pc = decltype(PC)::value;
      dma_m0<(c & 1) * XCH + pc * 1024>(ldsx, voffX[pc & 1], xgw + (size_t)pc * 8192 + c * 128); }); };
  if constexpr (SMALL) { tileW(IC<0>{}); tileW(IC<1>{}); tileW(IC<2>{}); } else { chunkX(IC<0>{}); tileW(IC<0>{}); tileW(IC<1>{}); chunkX(IC<1>{}); tileW(IC<2>{}); }
  {
#pragma unroll
    for (int f = 0; f < 2; ++f) {
      s16x8 v;
#pragma unroll
      for (int j = 0; j < 8; ++j) v[j] = (short)wsrc[(size_t)((j < 4 ? 4 * g + j : 12 + 4 * g + j)) * DM + f * 16 + li];
      *(s16x8*)(wdst + f * 1024 + lane * 16) = v;
    }
  }
  asm volatile("" ::: ACLOB);
  sfor<NRB * 8>([&](auto I) { acc_zero4<4 * decltype(I)::value>(); });
  s16x8 wf[2][8], xf[2][NRB];
  auto wread = [&](auto N, auto I) { constexpr int n = decltype(N)::value, i = decltype(I)::value;
    const unsigned a = (wofs[i >> 1] ^ ((i & 1) * 8)) + (n % 3) * TILE; wf[n & 1][i] = cat8(lds_tr(a), lds_tr(a + 1024)); };
  auto xread = [&](auto N, auto R) { constexpr int n = decltype(N)::value, rb = decltype(R)::value;
    xf[n & 1][rb] = lds_b128(xofs[n & 1] + ((n >> 1) & 1) * XCH + rb * 2048); };
  if constexpr (SMALL) WAITV(4); else WAITV(9);
  BARRIER();
  TS(tqa);
  sfor<NRB>([&](auto R) { xread(IC<0>{}, R); });
  sfor<8>([&](auto I) { wread(IC<0>{}, I); });
  auto qstep = [&](auto N, auto WSC) {
    constexpr int n = decltype(N)::value;
    constexpr int WN = SMALL ? decltype(WSC)::value : ((n & 1) ? (n <= 13 ? 2 : 0) : (n <= 12 ? 7 : 0));
    TS(tw0);
    asm volatile("s_waitcnt vmcnt(%0) lgkmcnt(0)" :: "i"(WN) : "memory");
    BARRIER();
    TS(tw1); tqw += tw1 - tw0;
    SB();
    if constexpr (n + 3 < 16) tileW(IC<n + 3>{});
    if constexpr (SMALL) {
      if constexpr (n < 4) { if constexpr ((n & 1) == 0) tileK(IC<n>{}, kg + (size_t)(n / 2) * 8192); else tileV(IC<n>{}, vg + (size_t)(n / 2) * 8192); }
      if constexpr (n == 13) tileV(IC<5>{}, vg + (size_t)2 * 8192);
    } else {
      if constexpr ((n & 1) == 1 && (n + 3) / 2 < 8) chunkX(IC<(n + 3) / 2>{});
    }
    SB();
    sfor<8 * NRB>([&](auto M) { constexpr int m = decltype(M)::value, d = m / NRB, rb = m % NRB;
      mfma_acc<AO(rb, d)>(wf[n & 1][d], xf[n & 1][rb]);
      if constexpr (n < 15) {
        if constexpr (m < NRB) xread(IC<n + 1>{}, IC<m>{});
        else if constexpr (m < NRB + 8) wread(IC<n + 1>{}, IC<m - NRB>{});
      }
      SB();
    });
  };
  qstep(IC<0>{}, IC<2>{}); qstep(IC<1>{}, IC<6>{}); qstep(IC<2>{}, IC<10>{}); qstep(IC<3>{}, IC<10>{});
  qstep(IC<4>{}, IC<10>{}); qstep(IC<5>{}, IC<6>{}); qstep(IC<6>{}, IC<2>{}); qstep(IC<7>{}, IC<2>{});
  qstep(IC<8>{}, IC<2>{}); qstep(IC<9>{}, IC<2>{}); qstep(IC<10>{}, IC<2>{}); qstep(IC<11>{}, IC<2>{});
  qstep(IC<12>{}, IC<2>{}); qstep(IC<13>{}, IC<2>{}); qstep(IC<14>{}, IC<4>{}); qstep(IC<15>{}, IC<4>{});
  TS(tqb);
  if constexpr (SMALL) tileK(IC<4>{}, kg + (size_t)2 * 8192);
  asm volatile("s_nop 7\n\ts_nop 7" ::: "memory");
  s16x8 Qv[4];
  sfor<NRB>([&](auto R) { sfor<4>([&](auto Tt) {
    constexpr int rb = decltype(R)::value, t = decltype(Tt)::value, f = rb * 4 + t;
    float v[8];
    sfor<8>([&](auto E) { v[decltype(E)::value] = acc_rd<AO(rb, 2 * t) + decltype(E)::value>() * QSCALE; });
    const unsigned c0 = cvtpk(v[0], v[1]), c1 = cvtpk(v[2], v[3]), c2 = cvtpk(v[4], v[5]), c3 = cvtpk(v[6], v[7]);
    const bool odd = (g & 1) != 0;
    const unsigned q0 = odd ? c2 : c0, q1 = odd ? c3 : c1, q2 = odd ? c0 : c2, q3 = odd ? c1 : c3;
    if constexpr (f < 8) { acc_wr<AQ(f)>(q0); acc_wr<AQ(f) + 1>(q1); acc_wr<AQ(f) + 2>(q2); acc_wr<AQ(f) + 3>(q3); }
    else { u32x4 q = {q0, q1, q2, q3}; Qv[f - 8] = __builtin_bit_cast(s16x8, q); }
  }); });
  sfor<NRB * 8>([&](auto I) { acc_zero4<4 * decltype(I)::value>(); });

  f32x4 NM[NRB], S[NRB][2];
  float lsum[NRB];
  unsigned Pw[NRB][4];
#pragma unroll
  for (int rb = 0; rb < NRB; ++rb) { lsum[rb] = 0.f; NM[rb] = f32x4{0.f, 0.f, 0.f, 0.f}; }
  auto rowmax = [&](const f32x4 (&Sr)[2]) {
    float t = max3f(Sr[0][0], Sr[0][1], Sr[0][2]);
    t = max3f(t, Sr[0][3], Sr[1][0]); t = max3f(t, Sr[1][1], Sr[1][2]); t = max2f(t, Sr[1][3]);
    return t;
  };
  auto xlane_max = [&](float t) { t = fmaxf(t, __shfl_xor(t, 16)); t = fmaxf(t, __shfl_xor(t, 32)); return t; };
  auto sm_item = [&](int rb, int k) {
    if (k < 8) { float t = __builtin_amdgcn_exp2f(S[rb][k >> 2][k & 3]); asm volatile("" : "+v"(t)); S[rb][k >> 2][k & 3] = t; }
    else if (k < 16) { float t = fadd_s(lsum[rb], S[rb][(k - 8) >> 2][(k - 8) & 3]); asm volatile("" : "+v"(t)); lsum[rb] = t; }
    else { const int c = k - 16; unsigned t = cvtpk(S[rb][c >> 1][(c & 1) * 2], S[rb][c >> 1][(c & 1) * 2 + 1]); asm volatile("" : "+v"(t)); Pw[rb][c] = t; }
  };
  auto Pf = [&](int rb) { u32x4 v = {Pw[rb][0], Pw[rb][1], Pw[rb][2], Pw[rb][3]}; return __builtin_bit_cast(s16x8, v); };

  TS(ts1);
  s16x8 vf[8];
  auto do_qk = [&](auto KSC, auto VSC, auto VRC) {
    constexpr int KS = decltype(KSC)::value, VS = decltype(VSC)::value; constexpr bool VR = decltype(VRC)::value != 0;
    s16x8 kf[2][4];
#pragma unroll
    for (int m = 0; m < 8; ++m) kf[m >> 2][m & 3] = lds_b128(kofs[m & 3] + KS * TILE + (m >> 2) * 4096);
    SB();
    sfor<8 * NRB>([&](auto M) {
      constexpr int m = decltype(M)::value, g8 = m / NRB, rb = m % NRB, kb = g8 >> 2, ks = g8 & 3, f = rb * 4 + ks;
      if constexpr (f < 8) { if constexpr (ks == 0) mfma_qk0<AQ(f)>(S[rb][kb], kf[kb][ks], NM[rb]); else mfma_qk<AQ(f)>(S[rb][kb], kf[kb][ks]); }
      else { if constexpr (ks == 0) mfma_v0(S[rb][kb], kf[kb][ks], Qv[f - 8], NM[rb]); else mfma_v(S[rb][kb], kf[kb][ks], Qv[f - 8]); }
      if constexpr (VR && rb == NRB - 1) { const unsigned a = vofs[g8] + VS * TILE; vf[g8] = cat8(lds_tr(a), lds_tr(a + 4096)); }
      SB();
    });
    if constexpr (NRB == 3) asm volatile("s_nop 7" : "+v"(S[0][0]), "+v"(S[0][1]), "+v"(S[1][0]), "+v"(S[1][1]), "+v"(S[2][0]), "+v"(S[2][1]));
    else asm volatile("s_nop 7" : "+v"(S[0][0]), "+v"(S[0][1]), "+v"(S[1][0]), "+v"(S[1][1]));
  };
  auto do_check = [&](bool first) {
    float ta = max2f(S[0][0][0], S[0][0][1]), tb = max2f(S[0][0][2], S[0][0][3]);
#pragma unroll
    for (int i = 1; i < 2 * NRB; ++i) { ta = max3f(ta, S[i >> 1][i & 1][0], S[i >> 1][i & 1][1]); tb = max3f(tb, S[i >> 1][i & 1][2], S[i >> 1][i & 1][3]); }
    if (__builtin_expect(first || __any(max2f(ta, tb) > THR), 0)) {
      sfor<NRB>([&](auto R) {
        constexpr int rb = decltype(R)::value;
        const float rm = xlane_max(rowmax(S[rb]));
        const float dl = first ? rm : fmaxf(rm, 0.f);
        const float al = first ? 1.f : __builtin_amdgcn_exp2f(-dl);
        S[rb][0] -= dl; S[rb][1] -= dl; NM[rb] -= dl; lsum[rb] *= al;
        sfor<32>([&](auto E) { acc_scale<AO(rb, 0) + decltype(E)::value>(al); });
      });
    }
  };
  auto do_smpv = [&]() {
#pragma unroll
    for (int k = 0; k < 20; ++k) sm_item(0, k);
    asm volatile("s_nop 1" ::: "memory");
    SB();
    sfor<8 * NRB>([&](auto M) {
      constexpr int m = decltype(M)::value, rb = m / 8, db = m % 8;
      mfma_acc<AO(rb, db)>(vf[db], Pf(rb));
      if constexpr (rb + 1 < NRB) {
        constexpr int k0 = db < 4 ? 2 * db : 8 + 3 * (db - 4), k1 = db < 4 ? 2 * db + 2 : 8 + 3 * (db - 3);
#pragma unroll
        for (int k = k0; k < k1; ++k) sm_item(rb + 1, k);
        if constexpr (db == 7) asm volatile("s_nop 1" ::: "memory");
      }
      SB();
    });
  };
  if constexpr (SMALL) {
#pragma unroll
    for (int rb = 0; rb < NRB; ++rb) { S[rb][0] = f32x4{-1e30f, -1e30f, -1e30f, -1e30f}; S[rb][1] = S[rb][0]; }
#pragma unroll
    for (int db = 0; db < 8; ++db) vf[db] = s16x8{0, 0, 0, 0, 0, 0, 0, 0};
  }
  auto wait_vf = [&]() { asm volatile("s_waitcnt lgkmcnt(0)" : "+v"(vf[0]), "+v"(vf[1]), "+v"(vf[2]), "+v"(vf[3]), "+v"(vf[4]), "+v"(vf[5]), "+v"(vf[6]), "+v"(vf[7]) :: "memory"); };
  auto aiter = [&](int j, auto PHC) {
    constexpr int PH = decltype(PHC)::value;
    constexpr int KS = 2 * PH, VS = 2 * PH + 1, KD = 2 * ((PH + 3) % 4), VD = KD + 1;
    TS(tw0);
    if constexpr (SMALL) { if (j < 30) WAITV(16); else WAITV(0); }
    BARRIER();
    TS(tw1); twsum += tw1 - tw0;
    if constexpr (SMALL) {
      if (j + 3 < 32) tileK(IC<KD>{}, kg + (size_t)(j + 3) * 8192);
      SB();
      do_smpv();
      do_qk(IC<KS>{}, IC<VS>{}, IC<1>{});
      do_check(j == 0);
      if (j + 3 < 32) tileV(IC<VD>{}, vg + (size_t)(j + 3) * 8192);
      wait_vf();
    } else {
      do_qk(IC<KS>{}, IC<VS>{}, IC<1>{});
      do_check(j == 0);
      do_smpv();
    }
  };
  for (int j = 0; j < 32; j += 4) { aiter(j, IC<0>{}); aiter(j + 1, IC<1>{}); aiter(j + 2, IC<2>{}); aiter(j + 3, IC<3>{}); }
  if constexpr (SMALL) do_smpv();

  TS(ts3);
  asm volatile("s_nop 7\n\ts_nop 7" ::: "memory");
  sfor<NRB>([&](auto R) {
    constexpr int rb = decltype(R)::value;
    float l = lsum[rb]; l += __shfl_xor(l, 16); l += __shfl_xor(l, 32);
    const float il = __builtin_amdgcn_rcpf(l);
    sfor<4>([&](auto Tt) {
      constexpr int t = decltype(Tt)::value;
      float v[8];
      sfor<8>([&](auto E) { v[decltype(E)::value] = acc_rd<AO(rb, 2 * t) + decltype(E)::value>() * il; });
      u32x4 o = {cvtpk(v[0], v[1]), cvtpk(v[2], v[3]), cvtpk(v[4], v[5]), cvtpk(v[6], v[7])};
      *(u32x4*)(ob + (size_t)rb * (NH * 4 * 1024) + t * 1024) = o;
    });
  });
}

__global__ __launch_bounds__(512, 2)
void attn_kernel(const __hip_bfloat16* __restrict__ xA, const __hip_bfloat16* __restrict__ kA, const __hip_bfloat16* __restrict__ vA, const __hip_bfloat16* __restrict__ wqA,
                 const __hip_bfloat16* __restrict__ xB, const __hip_bfloat16* __restrict__ kB, const __hip_bfloat16* __restrict__ vB, const __hip_bfloat16* __restrict__ wqB,
                 const __hip_bfloat16* __restrict__ woA, const __hip_bfloat16* __restrict__ woB, __hip_bfloat16* __restrict__ ows) {
  __shared__ __attribute__((aligned(16))) char smem[20 * TILE];
  const unsigned lds0 = (unsigned)(size_t)smem;
  const int tid = threadIdx.x, lane = tid & 63;
  const int w = __builtin_amdgcn_readfirstlane(tid >> 6);
  const int e = w & 1, r = (w >> 1) & 1;
  const int idx = blockIdx.x, xcd = idx & 7, j0 = idx >> 3, blk = j0 >> 5, jj = j0 & 31;
  const int b = (xcd >> 2) * 8 + (jj >> 2), h = ((xcd & 3) * 4 + (jj & 3)) * 2 + e;
  const char* xg = (const char*)(blk ? xB : xA) + (size_t)b * SQ * DM * 2;
  const char* wqg = (const char*)(blk ? wqB : wqA) + (size_t)h * DH * 2;
  const char* kg = (const char*)(blk ? kB : kA) + ((size_t)b * NH + h) * SK * DH * 2 + r * 4096;
  const char* vg = (const char*)(blk ? vB : vA) + ((size_t)b * NH + h) * SK * DH * 2 + r * 4096;
  const unsigned ldsh = lds0 + e * (8 * TILE);
  const int rbg = w < 4 ? 3 * r : 6 + 2 * r;
  char* ob = (char*)ows + ((((size_t)(blk * NB + b) * 10 + rbg) * NH + h) * 4) * 1024 + lane * 16;
  const int rp = idx & 255, rnt = rp & 7, rks = (rp >> 3) * 4 + (w >> 1);
  const unsigned short* wsrc = (const unsigned short*)((idx >> 8) ? woB : woA) + (size_t)rks * 32 * DM + rnt * 64 + (w & 1) * 32;
  char* wdst = (char*)ows + WS_WO + ((size_t)((idx >> 8) * 8 + rnt) * 128 + rks) * 4096 + (w & 1) * 2048;
  if (w < 4) attn_body<3>(wsrc, wdst, xg, wqg, kg, vg, ob, lds0, ldsh, r, w & 3, 16 * rbg, lane); else attn_body<2>(wsrc, wdst, xg, wqg, kg, vg, ob, lds0, ldsh, r, w & 3, 16 * rbg, lane);
}

#define ACLOB2 "a0","a1","a2","a3","a4","a5","a6","a7","a8","a9","a10","a11","a12","a13","a14","a15","a16","a17","a18","a19","a20","a21","a22","a23","a24","a25","a26","a27","a28","a29","a30","a31","a32","a33","a34","a35","a36","a37","a38","a39","a40","a41","a42","a43","a44","a45","a46","a47","a48","a49","a50","a51","a52","a53","a54","a55","a56","a57","a58","a59","a60","a61","a62","a63","a64","a65","a66","a67","a68","a69","a70","a71","a72","a73","a74","a75","a76","a77","a78","a79","a80","a81","a82","a83","a84","a85","a86","a87","a88","a89","a90","a91","a92","a93","a94","a95","a96","a97","a98","a99","a100","a101","a102","a103","a104","a105","a106","a107","a108","a109","a110","a111","a112","a113","a114","a115","a116","a117","a118","a119","a120","a121","a122","a123","a124","a125","a126","a127","a128","a129","a130","a131","a132","a133","a134","a135","a136","a137","a138","a139","a140","a141","a142","a143","a144","a145","a146","a147","a148","a149","a150","a151","a152","a153","a154","a155","a156","a157","a158","a159"
constexpr int K2_LDS = 4 * 30 * 1024;
__global__ __launch_bounds__(256, 1)
void oproj_kernel(const char* __restrict__ ws, __hip_bfloat16* __restrict__ out) {
  __shared__ __attribute__((aligned(16))) char smem[K2_LDS];
  const unsigned lds0 = (unsigned)(size_t)smem;
  const int tid = threadIdx.x, lane = tid & 63;
  const int kq = __builtin_amdgcn_readfirstlane(tid >> 6);
  const int li = lane & 15, g = lane >> 4;
  const int idx = blockIdx.x, xcd = idx & 7, r = idx >> 3;
  const int nt = r & 7, bb = xcd * 4 + (r >> 3);
  const int blk = bb >> 4;
  const char* og = ws + (size_t)bb * 10 * 128 * 1024 + (size_t)kq * 32 * 1024;
  const char* wg = ws + WS_WO + ((size_t)(blk * 8 + nt) * 128 + kq * 32) * 4096;
  unsigned ovoff[10];
#pragma unroll
  for (int rb = 0; rb < 10; ++rb) ovoff[rb] = lane * 16 + rb * 131072;

  asm volatile("" ::: ACLOB2);
  s16x8 of[3][10], wf[3][4];
  auto ld = [&](auto SLC, auto IT, int ks) {
    constexpr int sl = decltype(SLC)::value, it = decltype(IT)::value;
    if constexpr (it < 4) wf[sl][it] = gload16<it * 1024>(ovoff[0], wg + (size_t)ks * 4096);
    else of[sl][it - 4] = gload16<0>(ovoff[it - 4], og + (size_t)ks * 1024);
  };
  sfor<14>([&](auto I) { ld(IC<0>{}, I, 0); });
  sfor<14>([&](auto I) { ld(IC<1>{}, I, 1); });
  sfor<40>([&](auto I) { acc_zero4<4 * decltype(I)::value>(); });
  auto step = [&](auto PHC, int ks) {
    constexpr int PH = decltype(PHC)::value;
    const int kn = ks + 2 < 32 ? ks + 2 : 31;
    WAITV(14);
    asm volatile("" : "+v"(of[PH][0]), "+v"(of[PH][1]), "+v"(of[PH][2]), "+v"(of[PH][3]), "+v"(of[PH][4]), "+v"(of[PH][5]), "+v"(of[PH][6]), "+v"(of[PH][7]), "+v"(of[PH][8]), "+v"(of[PH][9]));
    asm volatile("" : "+v"(wf[PH][0]), "+v"(wf[PH][1]), "+v"(wf[PH][2]), "+v"(wf[PH][3]));
    SB();
    sfor<40>([&](auto M) { constexpr int m = decltype(M)::value, nb = m / 10, rb = m % 10;
      mfma_acc<(rb * 4 + nb) * 4>(wf[PH][nb], of[PH][rb]);
      if constexpr ((m & 1) == 0 && m / 2 < 14) ld(IC<(PH + 2) % 3>{}, IC<m / 2>{}, kn);
      SB();
    });
  };
  for (int ks = 0; ks < 30; ks += 3) { step(IC<0>{}, ks); step(IC<1>{}, ks + 1); step(IC<2>{}, ks + 2); }
  step(IC<0>{}, 30); step(IC<1>{}, 31);
  asm volatile("s_waitcnt vmcnt(0)\n\ts_nop 7\n\ts_nop 7" ::: "memory");
  f32x4 mine[10];
  sfor<4>([&](auto NBc) {
    constexpr int nb = decltype(NBc)::value;
    sfor<10>([&](auto R) {
      constexpr int rb = decltype(R)::value;
      f32x4 v = {acc_rd<(rb * 4 + nb) * 4>(), acc_rd<(rb * 4 + nb) * 4 + 1>(), acc_rd<(rb * 4 + nb) * 4 + 2>(), acc_rd<(rb * 4 + nb) * 4 + 3>()};
      if (nb == kq) mine[rb] = v;
      else { const int sl = (kq - nb - 1) & 3; *(lds_f32x4_ptr)(lds0 + ((nb * 3 + sl) * 10 + rb) * 1024 + lane * 16) = v; }
    });
  });
  __syncthreads();
  char* outp = (char*)out + (size_t)(bb & 15) * SQ * 2048 + blk * 1024 + nt * 128 + kq * 32 + g * 8;
#pragma unroll
  for (int rb = 0; rb < 10; ++rb) {
    f32x4 sacc = mine[rb];
#pragma unroll
    for (int sl = 0; sl < 3; ++sl) sacc += *(lds_f32x4_ptr)(lds0 + ((kq * 3 + sl) * 10 + rb) * 1024 + lane * 16);
    uint2 o2; o2.x = cvtpk(sacc[0], sacc[1]); o2.y = cvtpk(sacc[2], sacc[3]);
    *(uint2*)(outp + (size_t)(16 * rb + li) * 2048) = o2;
  }
}

extern "C" void kernel_launch(void* const* d_in, const int* in_sizes, int n_in,
                              void* d_out, int out_size, void* d_ws, size_t ws_size,
                              hipStream_t stream) {
  const __hip_bfloat16* xA = (const __hip_bfloat16*)d_in[0];
  const __hip_bfloat16* kA = (const __hip_bfloat16*)d_in[1];
  const __hip_bfloat16* vA = (const __hip_bfloat16*)d_in[2];
  const __hip_bfloat16* wqA = (const __hip_bfloat16*)d_in[3];
  const __hip_bfloat16* woA = (const __hip_bfloat16*)d_in[4];
  const __hip_bfloat16* xB = (const __hip_bfloat16*)d_in[5];
  const __hip_bfloat16* kB = (const __hip_bfloat16*)d_in[6];
  const __hip_bfloat16* vB = (const __hip_bfloat16*)d_in[7];
  const __hip_bfloat16* wqB = (const __hip_bfloat16*)d_in[8];
  const __hip_bfloat16* woB = (const __hip_bfloat16*)d_in[9];
  __hip_bfloat16* ows = (__hip_bfloat16*)d_ws;
  attn_kernel<<<512, 512, 0, stream>>>(xA, kA, vA, wqA, xB, kB, vB, wqB, woA, woB, ows);
  oproj_kernel<<<256, 256, 0, stream>>>((const char*)d_ws, (__hip_bfloat16*)d_out);
}
```

```cpp
#include <hip/hip_runtime.h>
#include <hip/hip_bf16.h>
#include <utility>
#include <type_traits>

typedef __attribute__((ext_vector_type(8))) short s16x8;
typedef __attribute__((ext_vector_type(4))) short s16x4;
typedef __attribute__((ext_vector_type(4))) float f32x4;
typedef __attribute__((ext_vector_type(2))) float f32x2;
typedef __attribute__((ext_vector_type(2))) __bf16 bf16x2;
typedef __attribute__((ext_vector_type(4))) unsigned u32x4;
typedef __attribute__((address_space(3))) s16x4* lds_s16x4_ptr;
typedef __attribute__((address_space(3))) s16x8* lds_s16x8_ptr;
typedef __attribute__((address_space(3))) f32x4* lds_f32x4_ptr;

#define DI __device__ __forceinline__

constexpr int NB = 16, SQ = 160, DM = 512, NH = 32, SK = 1024, DH = 128;
constexpr int NS = 5;
constexpr int SLOT = 16384;
constexpr float QSCALE = 0.088388347648318440f * 1.4426950408889634f;
constexpr size_t WS_WO = (size_t)48 << 20;
constexpr float THR = 8.0f;

DI unsigned cvtpk(float lo, float hi) { f32x2 v = {lo, hi}; bf16x2 b = __builtin_convertvector(v, bf16x2); return __builtin_bit_cast(unsigned, b); }
DI s16x8 pack8(f32x4 a, f32x4 b) { u32x4 w = {cvtpk(a[0], a[1]), cvtpk(a[2], a[3]), cvtpk(b[0], b[1]), cvtpk(b[2], b[3])}; return __builtin_bit_cast(s16x8, w); }
DI float max3f(float a, float b, float c) { float r; asm("v_max3_f32 %0, %1, %2, %3" : "=v"(r) : "v"(a), "v"(b), "v"(c)); return r; }
DI float max2f(float a, float b) { float r; asm("v_max_f32_e32 %0, %1, %2" : "=v"(r) : "v"(a), "v"(b)); return r; }
DI f32x4 mfma16(s16x8 a, s16x8 b, f32x4 c) { return __builtin_amdgcn_mfma_f32_16x16x32_bf16(a, b, c, 0, 0, 0); }
DI s16x8 lds_b128(unsigned addr) { return *(lds_s16x8_ptr)(addr); }
DI s16x4 lds_tr(unsigned addr) { return __builtin_amdgcn_ds_read_tr16_b64_v4i16((lds_s16x4_ptr)(addr)); }
DI s16x8 cat8(s16x4 a, s16x4 b) { return __builtin_shufflevector(a, b, 0, 1, 2, 3, 4, 5, 6, 7); }

DI void dma4(unsigned ldsaddr, unsigned v0, unsigned v1, unsigned v2, unsigned v3, const void* gbase) {
  asm volatile("s_mov_b32 m0, %0\n\ts_nop 0\n\t"
               "global_load_lds_dwordx4 %1, %5 nt\n\t"
               "global_load_lds_dwordx4 %2, %5 offset:1024 nt\n\t"
               "global_load_lds_dwordx4 %3, %5 offset:2048 nt\n\t"
               "global_load_lds_dwordx4 %4, %5 offset:3072 nt"
               :: "s"(ldsaddr), "v"(v0), "v"(v1), "v"(v2), "v"(v3), "s"(gbase) : "memory");
}
DI void dma1(unsigned ldsaddr, unsigned v0, const void* gbase) {
  asm volatile("s_mov_b32 m0, %0\n\ts_nop 0\n\t"
               "global_load_lds_dwordx4 %1, %2"
               :: "s"(ldsaddr), "v"(v0), "s"(gbase) : "memory");
}
template <int OFF> DI s16x8 gload16(unsigned voff, const void* gbase) {
  s16x8 r; asm volatile("global_load_dwordx4 %0, %1, %2 offset:%3" : "=v"(r) : "v"(voff), "s"(gbase), "i"(OFF) : "memory"); return r;
}
template <int OFF> DI unsigned gload_u16(unsigned voff, const void* gbase) {
  unsigned r; asm volatile("global_load_ushort %0, %1, %2 offset:%3" : "=v"(r) : "v"(voff), "s"(gbase), "i"(OFF) : "memory"); return r;
}
#define WAITV(n) asm volatile("s_waitcnt vmcnt(" #n ")" ::: "memory")
#define BARRIER() asm volatile("s_barrier" ::: "memory")
#define PIN5(a) asm volatile("" : "+v"(a[0]), "+v"(a[1]), "+v"(a[2]), "+v"(a[3]), "+v"(a[4]))

#define ACLOB "a0","a1","a2","a3","a4","a5","a6","a7","a8","a9","a10","a11","a12","a13","a14","a15","a16","a17","a18","a19","a20","a21","a22","a23","a24","a25","a26","a27","a28","a29","a30","a31","a32","a33","a34","a35","a36","a37","a38","a39","a40","a41","a42","a43","a44","a45","a46","a47","a48","a49","a50","a51","a52","a53","a54","a55","a56","a57","a58","a59","a60","a61","a62","a63","a64","a65","a66","a67","a68","a69","a70","a71","a72","a73","a74","a75","a76","a77","a78","a79","a80","a81","a82","a83","a84","a85","a86","a87","a88","a89","a90","a91","a92","a93","a94","a95","a96","a97","a98","a99","a100","a101","a102","a103","a104","a105","a106","a107","a108","a109","a110","a111","a112","a113","a114","a115","a116","a117","a118","a119","a120","a121","a122","a123","a124","a125","a126","a127"
#define SB() __builtin_amdgcn_sched_barrier(0)
template <int... I, class F> DI void sfor_impl(std::integer_sequence<int, I...>, F&& f) { (f(std::integral_constant<int, I>{}), ...); }
template <int N, class F> DI void sfor(F&& f) { sfor_impl(std::make_integer_sequence<int, N>{}, f); }
template <int N> using IC = std::integral_constant<int, N>;
constexpr int AO(int rb, int db) { return (rb * 8 + db) * 4; }
constexpr int AQ(int f) { return 96 + f * 4; }
template <int A> DI void mfma_acc(s16x8 a, s16x8 b) {
  asm volatile("v_mfma_f32_16x16x32_bf16 a[%c2:%c3], %0, %1, a[%c2:%c3]" :: "v"(a), "v"(b), "i"(A), "i"(A + 3));
}
template <int A> DI void mfma_qk0(f32x4& S, s16x8 kf, const f32x4& nm) {
  asm volatile("v_mfma_f32_16x16x32_bf16 %0, %1, a[%c3:%c4], %2" : "=&v"(S) : "v"(kf), "v"(nm), "i"(A), "i"(A + 3));
}
template <int A> DI void mfma_qk(f32x4& S, s16x8 kf) {
  asm volatile("v_mfma_f32_16x16x32_bf16 %0, %1, a[%c2:%c3], %0" : "+v"(S) : "v"(kf), "i"(A), "i"(A + 3));
}
DI void mfma_v0(f32x4& D, s16x8 a, s16x8 b, const f32x4& c) { asm volatile("v_mfma_f32_16x16x32_bf16 %0, %1, %2, %3" : "=&v"(D) : "v"(a), "v"(b), "v"(c)); }
DI void mfma_v(f32x4& D, s16x8 a, s16x8 b) { asm volatile("v_mfma_f32_16x16x32_bf16 %0, %1, %2, %0" : "+v"(D) : "v"(a), "v"(b)); }
template <int A> DI float acc_rd() { float r; asm volatile("v_accvgpr_read_b32 %0, a[%c1]" : "=v"(r) : "i"(A)); return r; }
template <int A> DI void acc_wr(unsigned v) { asm volatile("v_accvgpr_write_b32 a[%c1], %0" :: "v"(v), "i"(A)); }
template <int A> DI void acc_zero4() { asm volatile("v_accvgpr_write_b32 a[%c0], 0\n\tv_accvgpr_write_b32 a[%c1], 0\n\tv_accvgpr_write_b32 a[%c2], 0\n\tv_accvgpr_write_b32 a[%c3], 0" :: "i"(A), "i"(A + 1), "i"(A + 2), "i"(A + 3)); }
template <int A> DI void acc_scale(float f) { float t; asm volatile("v_accvgpr_read_b32 %0, a[%c2]\n\tv_mul_f32 %0, %0, %1\n\tv_accvgpr_write_b32 a[%c2], %0" : "=&v"(t) : "v"(f), "i"(A)); }
DI float fadd_s(float a, float b) { float r; asm("v_add_f32_e32 %0, %1, %2" : "=v"(r) : "v"(a), "v"(b)); return r; }
template <int LOFF> DI void dma4_nt(unsigned ldsw, unsigned v0, unsigned v1, unsigned v2, unsigned v3, const void* gbase) {
  asm volatile("s_add_u32 m0, %0, %6\n\ts_nop 0\n\t"
               "global_load_lds_dwordx4 %1, %5 nt\n\t"
               "global_load_lds_dwordx4 %2, %5 offset:1024 nt\n\t"
               "global_load_lds_dwordx4 %3, %5 offset:2048 nt\n\t"
               "global_load_lds_dwordx4 %4, %5 offset:3072 nt"
               :: "s"(ldsw), "v"(v0), "v"(v1), "v"(v2), "v"(v3), "s"(gbase), "i"(LOFF) : "memory", "scc");
}
template <int LOFF> DI void dma_m0(unsigned ldsw, unsigned v0, const void* gbase) {
  asm volatile("s_add_u32 m0, %0, %3\n\ts_nop 0\n\tglobal_load_lds_dwordx4 %1, %2" :: "s"(ldsw), "v"(v0), "s"(gbase), "i"(LOFF) : "memory", "scc");
}
#define TS(v) do { } while (0)
constexpr int TILE = 8192;
constexpr int XREG = 120 * 1024, XCH = 20480;

template <int NRB>
DI void attn_body(const unsigned short* wsrc, char* wdst, const char* xg, const char* wqg, const char* kg, const char* vg, char* ob, const unsigned lds0, const unsigned ldsh, const int r, const int bw, const int row0, const int lane
                  ) {
  constexpr bool SMALL = NRB == 2;
  const int li = lane & 15, g = lane >> 4, q4 = (lane >> 2) & 3, p4 = lane & 3;
  const unsigned ldsw = ldsh + r * 4096;
  unsigned voffK[4], voffV[2];
  const int hw = SMALL ? 2 + r : r;
#pragma unroll
  for (int n = 0; n < 4; ++n) voffK[n] = g * 256 + (((li ^ g) ^ (4 * n)) * 16);
#pragma unroll
  for (int n = 0; n < 2; ++n) voffV[n] = g * 256 + ((li ^ (g << 1) ^ (n << 3)) * 16);
  const unsigned voffW = g * 8192 + ((li ^ ((g & 1) | ((g >> 1) << 2) | (r << 3))) * 16);
  const unsigned ldsq = ldsh + hw * 2048;
  const char* wqw = wqg + (size_t)hw * 8 * 8192;
  unsigned wofs[4];
#pragma unroll
  for (int t = 0; t < 4; ++t) wofs[t] = ldsh + (8 * g + q4) * 256 + (((4 * t + p4) ^ ((q4 & 1) | ((q4 >> 1) << 2) | ((g & 1) << 3))) * 16) + (p4 & 1) * 8;
  unsigned voffX[2], xofs[2];
#pragma unroll
  for (int n = 0; n < 2; ++n) voffX[n] = (lane >> 3) * 1024 + (((lane & 7) ^ (4 * ((n + bw) & 1) + g)) * 16);
#pragma unroll
  for (int kk = 0; kk < 2; ++kk) xofs[kk] = lds0 + XREG + (row0 + li) * 128 + (((4 * kk + g) ^ (li >> 1)) * 16);

  unsigned long long tqa = 0, tqb = 0; (void)tqa; (void)tqb;
  unsigned long long ts0 = 0, ts1 = 0, ts3 = 0, ts4 = 0, tw0 = 0, tw1 = 0, twsum = 0, tqw = 0;
  (void)ts0; (void)ts1; (void)ts3; (void)ts4; (void)tw0; (void)tw1; (void)twsum; (void)tqw;
  TS(ts0);

  auto pieceW = [&](auto N, auto I) { constexpr int n = decltype(N)::value, i = decltype(I)::value;
    dma_m0<(n % 7) * TILE + i * 1024>(ldsq, voffW, wqw + (size_t)(32 * n) * 8192 + (size_t)i * 32768); };
  auto tileW = [&](auto N) { pieceW(N, IC<0>{}); pieceW(N, IC<1>{}); };
  auto tileK = [&](auto SL, const char* gb) { dma4_nt<decltype(SL)::value * TILE>(ldsw, voffK[0], voffK[1], voffK[2], voffK[3], gb); };
  auto tileV = [&](auto SL, const char* gb) { dma4_nt<decltype(SL)::value * TILE>(ldsw, voffV[0], voffV[1], voffV[0], voffV[1], gb); };

  const unsigned ldsx = lds0 + XREG + bw * 5120;
  const char* xgw = xg + (size_t)bw * 40 * 1024;
  auto pieceX = [&](auto C, auto PC) { constexpr int c = decltype(C)::value, pc = decltype(PC)::value;
    dma_m0<(c & 1) * XCH + pc * 1024>(ldsx, voffX[pc & 1], xgw + (size_t)pc * 8192 + c * 128); };
  auto chunkX = [&](auto C) { sfor<5>([&](auto PC) { pieceX(C, PC); }); };
  if constexpr (!SMALL) chunkX(IC<0>{});
  tileW(IC<0>{}); tileW(IC<1>{});
  if constexpr (!SMALL) chunkX(IC<1>{});
  tileW(IC<2>{}); tileW(IC<3>{}); tileW(IC<4>{}); tileW(IC<5>{}); tileW(IC<6>{});
  asm volatile("" ::: ACLOB);
  sfor<NRB * 8>([&](auto I) { acc_zero4<4 * decltype(I)::value>(); });
  s16x8 wf[2][8], xf[2][NRB];
  auto wread = [&](auto N, auto I) { constexpr int n = decltype(N)::value, i = decltype(I)::value;
    const unsigned a = (wofs[i >> 1] ^ ((i & 1) * 8)) + (n % 7) * TILE; wf[n & 1][i] = cat8(lds_tr(a), lds_tr(a + 1024)); };
  auto xread = [&](auto N, auto R) { constexpr int n = decltype(N)::value, rb = decltype(R)::value;
    xf[n & 1][rb] = lds_b128(xofs[n & 1] + ((n >> 1) & 1) * XCH + rb * 2048); };
  if constexpr (SMALL) WAITV(12); else WAITV(17);
  BARRIER();
  TS(tqa);
  sfor<NRB>([&](auto R) { xread(IC<0>{}, R); });
  sfor<8>([&](auto I) { wread(IC<0>{}, I); });
  auto qstep = [&](auto N, auto WSC) {
    constexpr int n = decltype(N)::value;
    constexpr int WN = SMALL ? decltype(WSC)::value : (n == 0 ? 15 : n == 1 ? 10 : n == 2 ? 15 : (n & 1) ? (n <= 9 ? 2 : 0) : 63);
    TS(tw0);
    asm volatile("s_waitcnt vmcnt(%0) lgkmcnt(0)" :: "i"(WN) : "memory");
    BARRIER();
    TS(tw1); tqw += tw1 - tw0;
    SB();
    constexpr bool hasW = n + 7 < 16, hasX = !SMALL && (n & 1) == 1 && (n + 3) / 2 < 8, hasKV = SMALL && n >= 9 && n <= 14;
    sfor<8 * NRB>([&](auto M) { constexpr int m = decltype(M)::value, d = m / NRB, rb = m % NRB;
      mfma_acc<AO(rb, d)>(wf[n & 1][d], xf[n & 1][rb]);
      if constexpr (n < 15) {
        if constexpr (m < NRB) xread(IC<n + 1>{}, IC<m>{});
        else if constexpr (m < NRB + 8) wread(IC<n + 1>{}, IC<m - NRB>{});
      }
      if constexpr (hasW && (m == 1 || m == 3)) pieceW(IC<hasW ? n + 7 : 0>{}, IC<m / 2>{});
      if constexpr (hasX && m >= 5 && m <= 13 && (m & 1)) pieceX(IC<hasX ? (n + 3) / 2 : 0>{}, IC<(m - 5) / 2>{});
      if constexpr (hasKV && m == 1) {
        constexpr int t = n - 9, sl = 2 * ((t / 2 + 1) % 4) + (t & 1);
        if constexpr ((t & 1) == 0) tileK(IC<sl>{}, kg + (size_t)(t / 2) * 8192); else tileV(IC<sl>{}, vg + (size_t)(t / 2) * 8192);
      }
      SB();
    });
  };
  qstep(IC<0>{}, IC<10>{}); qstep(IC<1>{}, IC<10>{}); qstep(IC<2>{}, IC<10>{}); qstep(IC<3>{}, IC<10>{});
  qstep(IC<4>{}, IC<10>{}); qstep(IC<5>{}, IC<10>{}); qstep(IC<6>{}, IC<10>{}); qstep(IC<7>{}, IC<10>{});
  qstep(IC<8>{}, IC<10>{}); qstep(IC<9>{}, IC<10>{}); qstep(IC<10>{}, IC<12>{}); qstep(IC<11>{}, IC<14>{});
  qstep(IC<12>{}, IC<16>{}); qstep(IC<13>{}, IC<18>{}); qstep(IC<14>{}, IC<20>{}); qstep(IC<15>{}, IC<63>{});
  TS(tqb);
  asm volatile("s_nop 7\n\ts_nop 7" ::: "memory");
  s16x8 Qv[4];
  sfor<NRB>([&](auto R) { sfor<4>([&](auto Tt) {
    constexpr int rb = decltype(R)::value, t = decltype(Tt)::value, f = rb * 4 + t;
    float v[8];
    sfor<8>([&](auto E) { v[decltype(E)::value] = acc_rd<AO(rb, 2 * t) + decltype(E)::value>() * QSCALE; });
    const unsigned c0 = cvtpk(v[0], v[1]), c1 = cvtpk(v[2], v[3]), c2 = cvtpk(v[4], v[5]), c3 = cvtpk(v[6], v[7]);
    const bool odd = (g & 1) != 0;
    const unsigned q0 = odd ? c2 : c0, q1 = odd ? c3 : c1, q2 = odd ? c0 : c2, q3 = odd ? c1 : c3;
    if constexpr (f < 8) { acc_wr<AQ(f)>(q0); acc_wr<AQ(f) + 1>(q1); acc_wr<AQ(f) + 2>(q2); acc_wr<AQ(f) + 3>(q3); }
    else { u32x4 q = {q0, q1, q2, q3}; Qv[f - 8] = __builtin_bit_cast(s16x8, q); }
  }); });
  sfor<NRB * 8>([&](auto I) { acc_zero4<4 * decltype(I)::value>(); });

  unsigned kofs[4], vofs[8];
  {
    int l2 = lane; asm volatile("" : "+v"(l2));
    const int li2 = l2 & 15, g2 = l2 >> 4, q2 = (l2 >> 2) & 3, p2 = l2 & 3;
#pragma unroll
    for (int ks = 0; ks < 4; ++ks) kofs[ks] = ldsh + li2 * 256 + (((4 * ks + g2) ^ li2) * 16);
    const unsigned r7 = 4 * (g2 & 1) + q2;
#pragma unroll
    for (int db = 0; db < 8; ++db) vofs[db] = ldsh + (4 * g2 + q2) * 256 + (r7 ^ db) * 32 + (p2 >> 1) * 16 + (p2 & 1) * 8;
  }
  f32x4 NM[NRB], S[NRB][2];
  float lsum[NRB];
  unsigned Pw[NRB][4];
#pragma unroll
  for (int rb = 0; rb < NRB; ++rb) { lsum[rb] = 0.f; NM[rb] = f32x4{0.f, 0.f, 0.f, 0.f}; }
  auto rowmax = [&](const f32x4 (&Sr)[2]) {
    float t = max3f(Sr[0][0], Sr[0][1], Sr[0][2]);
    t = max3f(t, Sr[0][3], Sr[1][0]); t = max3f(t, Sr[1][1], Sr[1][2]); t = max2f(t, Sr[1][3]);
    return t;
  };
  auto xlane_max = [&](float t) { t = fmaxf(t, __shfl_xor(t, 16)); t = fmaxf(t, __shfl_xor(t, 32)); return t; };
  auto sm_item = [&](int rb, int k) {
    if (k < 8) { float t = __builtin_amdgcn_exp2f(S[rb][k >> 2][k & 3]); asm volatile("" : "+v"(t)); S[rb][k >> 2][k & 3] = t; }
    else if (k < 16) { float t = fadd_s(lsum[rb], S[rb][(k - 8) >> 2][(k - 8) & 3]); asm volatile("" : "+v"(t)); lsum[rb] = t; }
    else { const int c = k - 16; unsigned t = cvtpk(S[rb][c >> 1][(c & 1) * 2], S[rb][c >> 1][(c & 1) * 2 + 1]); asm volatile("" : "+v"(t)); Pw[rb][c] = t; }
  };
  auto Pf = [&](int rb) { u32x4 v = {Pw[rb][0], Pw[rb][1], Pw[rb][2], Pw[rb][3]}; return __builtin_bit_cast(s16x8, v); };

  TS(ts1);
  s16x8 vf[8];
  auto do_qk = [&](auto KSC, auto VSC, auto VRC) {
    constexpr int KS = decltype(KSC)::value, VS = decltype(VSC)::value; constexpr bool VR = decltype(VRC)::value != 0;
    s16x8 kf[2][4];
#pragma unroll
    for (int m = 0; m < 8; ++m) kf[m >> 2][m & 3] = lds_b128(kofs[m & 3] + KS * TILE + (m >> 2) * 4096);
    SB();
    sfor<8 * NRB>([&](auto M) {
      constexpr int m = decltype(M)::value, g8 = m / NRB, rb = m % NRB, kb = g8 >> 2, ks = g8 & 3, f = rb * 4 + ks;
      if constexpr (f < 8) { if constexpr (ks == 0) mfma_qk0<AQ(f)>(S[rb][kb], kf[kb][ks], NM[rb]); else mfma_qk<AQ(f)>(S[rb][kb], kf[kb][ks]); }
      else { if constexpr (ks == 0) mfma_v0(S[rb][kb], kf[kb][ks], Qv[f - 8], NM[rb]); else mfma_v(S[rb][kb], kf[kb][ks], Qv[f - 8]); }
      if constexpr (VR && rb == NRB - 1) { const unsigned a = vofs[g8] + VS * TILE; vf[g8] = cat8(lds_tr(a), lds_tr(a + 4096)); }
      SB();
    });
    if constexpr (NRB == 3) asm volatile("s_nop 7" : "+v"(S[0][0]), "+v"(S[0][1]), "+v"(S[1][0]), "+v"(S[1][1]), "+v"(S[2][0]), "+v"(S[2][1]));
    else asm volatile("s_nop 7" : "+v"(S[0][0]), "+v"(S[0][1]), "+v"(S[1][0]), "+v"(S[1][1]));
  };
  auto do_check = [&](bool first) {
    float ta = max2f(S[0][0][0], S[0][0][1]), tb = max2f(S[0][0][2], S[0][0][3]);
#pragma unroll
    for (int i = 1; i < 2 * NRB; ++i) { ta = max3f(ta, S[i >> 1][i & 1][0], S[i >> 1][i & 1][1]); tb = max3f(tb, S[i >> 1][i & 1][2], S[i >> 1][i & 1][3]); }
    if (__builtin_expect(first || __any(max2f(ta, tb) > THR), 0)) {
      sfor<NRB>([&](auto R) {
        constexpr int rb = decltype(R)::value;
        const float rm = xlane_max(rowmax(S[rb]));
        const float dl = first ? rm : fmaxf(rm, 0.f);
        const float al = first ? 1.f : __builtin_amdgcn_exp2f(-dl);
        S[rb][0] -= dl; S[rb][1] -= dl; NM[rb] -= dl; lsum[rb] *= al;
        sfor<32>([&](auto E) { acc_scale<AO(rb, 0) + decltype(E)::value>(al); });
      });
    }
  };
  auto do_smpv = [&]() {
#pragma unroll
    for (int k = 0; k < 20; ++k) sm_item(0, k);
    asm volatile("s_nop 1" ::: "memory");
    SB();
    sfor<8 * NRB>([&](auto M) {
      constexpr int m = decltype(M)::value, rb = m / 8, db = m % 8;
      mfma_acc<AO(rb, db)>(vf[db], Pf(rb));
      if constexpr (rb + 1 < NRB) {
        constexpr int k0 = db < 4 ? 2 * db : 8 + 3 * (db - 4), k1 = db < 4 ? 2 * db + 2 : 8 + 3 * (db - 3);
#pragma unroll
        for (int k = k0; k < k1; ++k) sm_item(rb + 1, k);
        if constexpr (db == 7) asm volatile("s_nop 1" ::: "memory");
      }
      SB();
    });
  };
  if constexpr (SMALL) {
#pragma unroll
    for (int rb = 0; rb < NRB; ++rb) { S[rb][0] = f32x4{-1e30f, -1e30f, -1e30f, -1e30f}; S[rb][1] = S[rb][0]; }
#pragma unroll
    for (int db = 0; db < 8; ++db) vf[db] = s16x8{0, 0, 0, 0, 0, 0, 0, 0};
  }
  auto wait_vf = [&]() { asm volatile("s_waitcnt lgkmcnt(0)" : "+v"(vf[0]), "+v"(vf[1]), "+v"(vf[2]), "+v"(vf[3]), "+v"(vf[4]), "+v"(vf[5]), "+v"(vf[6]), "+v"(vf[7]) :: "memory"); };
  auto aiter = [&](int j, auto PHC) {
    constexpr int PH = decltype(PHC)::value;
    constexpr int KS = 2 * ((PH + 1) % 4), VS = KS + 1, KD = 2 * PH, VD = KD + 1;
    TS(tw0);
    if constexpr (SMALL) { if (j < 30) WAITV(16); else WAITV(0); }
    BARRIER();
    TS(tw1); twsum += tw1 - tw0;
    if constexpr (SMALL) {
      if (j + 3 < 32) tileK(IC<KD>{}, kg + (size_t)(j + 3) * 8192);
      SB();
      do_smpv();
      do_qk(IC<KS>{}, IC<VS>{}, IC<1>{});
      do_check(j == 0);
      if (j + 3 < 32) tileV(IC<VD>{}, vg + (size_t)(j + 3) * 8192);
      wait_vf();
    } else {
      do_qk(IC<KS>{}, IC<VS>{}, IC<1>{});
      do_check(j == 0);
      do_smpv();
    }
  };
  for (int j = 0; j < 32; j += 4) { aiter(j, IC<0>{}); aiter(j + 1, IC<1>{}); aiter(j + 2, IC<2>{}); aiter(j + 3, IC<3>{}); }
  unsigned rp[2][8];
  {
    const unsigned ro0 = (4 * g) * 1024 + li * 2, ro1 = ro0 + 16384;
    sfor<2>([&](auto F) { sfor<8>([&](auto J) {
      constexpr int f = decltype(F)::value, j = decltype(J)::value;
      rp[f][j] = gload_u16<(j & 3) * 1024 + f * 32>(j < 4 ? ro0 : ro1, wsrc);
    }); });
  }
  if constexpr (SMALL) do_smpv();

  TS(ts3);
  asm volatile("s_nop 7\n\ts_nop 7" ::: "memory");
  sfor<NRB>([&](auto R) {
    constexpr int rb = decltype(R)::value;
    float l = lsum[rb]; l += __shfl_xor(l, 16); l += __shfl_xor(l, 32);
    const float il = __builtin_amdgcn_rcpf(l);
    sfor<4>([&](auto Tt) {
      constexpr int t = decltype(Tt)::value;
      float v[8];
      sfor<8>([&](auto E) { v[decltype(E)::value] = acc_rd<AO(rb, 2 * t) + decltype(E)::value>() * il; });
      u32x4 o = {cvtpk(v[0], v[1]), cvtpk(v[2], v[3]), cvtpk(v[4], v[5]), cvtpk(v[6], v[7])};
      *(u32x4*)(ob + (size_t)rb * (NH * 4 * 1024) + t * 1024) = o;
    });
  });
  asm volatile("s_waitcnt vmcnt(%c16)" : "+v"(rp[0][0]), "+v"(rp[0][1]), "+v"(rp[0][2]), "+v"(rp[0][3]), "+v"(rp[0][4]), "+v"(rp[0][5]), "+v"(rp[0][6]), "+v"(rp[0][7]),
               "+v"(rp[1][0]), "+v"(rp[1][1]), "+v"(rp[1][2]), "+v"(rp[1][3]), "+v"(rp[1][4]), "+v"(rp[1][5]), "+v"(rp[1][6]), "+v"(rp[1][7]) : "i"(NRB * 4) : "memory");
#pragma unroll
  for (int f = 0; f < 2; ++f) {
    u32x4 v = {rp[f][0] | (rp[f][1] << 16), rp[f][2] | (rp[f][3] << 16), rp[f][4] | (rp[f][5] << 16), rp[f][6] | (rp[f][7] << 16)};
    *(u32x4*)(wdst + f * 1024 + lane * 16) = v;
  }
}

__global__ __launch_bounds__(512, 2)
void attn_kernel(const __hip_bfloat16* __restrict__ xA, const __hip_bfloat16* __restrict__ kA, const __hip_bfloat16* __restrict__ vA, const __hip_bfloat16* __restrict__ wqA,
                 const __hip_bfloat16* __restrict__ xB, const __hip_bfloat16* __restrict__ kB, const __hip_bfloat16* __restrict__ vB, const __hip_bfloat16* __restrict__ wqB,
                 const __hip_bfloat16* __restrict__ woA, const __hip_bfloat16* __restrict__ woB, __hip_bfloat16* __restrict__ ows) {
  __shared__ __attribute__((aligned(16))) char smem[20 * TILE];
  const unsigned lds0 = (unsigned)(size_t)smem;
  const int tid = threadIdx.x, lane = tid & 63;
  const int w = __builtin_amdgcn_readfirstlane(tid >> 6);
  const int e = w & 1, r = (w >> 1) & 1;
  const int idx = blockIdx.x, xcd = idx & 7, j0 = idx >> 3, blk = j0 >> 5, jj = j0 & 31;
  const int b = (xcd >> 2) * 8 + (jj >> 2), h = ((xcd & 3) * 4 + (jj & 3)) * 2 + e;
  const char* xg = (const char*)(blk ? xB : xA) + (size_t)b * SQ * DM * 2;
  const char* wqg = (const char*)(blk ? wqB : wqA) + (size_t)h * DH * 2;
  const char* kg = (const char*)(blk ? kB : kA) + ((size_t)b * NH + h) * SK * DH * 2 + r * 4096;
  const char* vg = (const char*)(blk ? vB : vA) + ((size_t)b * NH + h) * SK * DH * 2 + r * 4096;
  const unsigned ldsh = lds0 + e * (8 * TILE);
  const int rbg = w < 4 ? 3 * r : 6 + 2 * r;
  char* ob = (char*)ows + ((((size_t)(blk * NB + b) * 10 + rbg) * NH + h) * 4) * 1024 + lane * 16;
  const int rpp = idx & 255, rnt = rpp & 7, rks = (rpp >> 3) * 4 + (w >> 1);
  const unsigned short* wsrc = (const unsigned short*)((idx >> 8) ? woB : woA) + (size_t)rks * 32 * DM + rnt * 64 + (w & 1) * 32;
  char* wdst = (char*)ows + WS_WO + ((size_t)((idx >> 8) * 8 + rnt) * 128 + rks) * 4096 + (w & 1) * 2048;
  if (w < 4) attn_body<3>(wsrc, wdst, xg, wqg, kg, vg, ob, lds0, ldsh, r, w & 3, 16 * rbg, lane); else attn_body<2>(wsrc, wdst, xg, wqg, kg, vg, ob, lds0, ldsh, r, w & 3, 16 * rbg, lane);
}

#define ACLOB2 "a0","a1","a2","a3","a4","a5","a6","a7","a8","a9","a10","a11","a12","a13","a14","a15","a16","a17","a18","a19","a20","a21","a22","a23","a24","a25","a26","a27","a28","a29","a30","a31","a32","a33","a34","a35","a36","a37","a38","a39","a40","a41","a42","a43","a44","a45","a46","a47","a48","a49","a50","a51","a52","a53","a54","a55","a56","a57","a58","a59","a60","a61","a62","a63","a64","a65","a66","a67","a68","a69","a70","a71","a72","a73","a74","a75","a76","a77","a78","a79","a80","a81","a82","a83","a84","a85","a86","a87","a88","a89","a90","a91","a92","a93","a94","a95","a96","a97","a98","a99","a100","a101","a102","a103","a104","a105","a106","a107","a108","a109","a110","a111","a112","a113","a114","a115","a116","a117","a118","a119","a120","a121","a122","a123","a124","a125","a126","a127","a128","a129","a130","a131","a132","a133","a134","a135","a136","a137","a138","a139","a140","a141","a142","a143","a144","a145","a146","a147","a148","a149","a150","a151","a152","a153","a154","a155","a156","a157","a158","a159"
constexpr int K2_LDS = 4 * 30 * 1024;
__global__ __launch_bounds__(256, 1)
void oproj_kernel(const char* __restrict__ ws, __hip_bfloat16* __restrict__ out) {
  __shared__ __attribute__((aligned(16))) char smem[K2_LDS];
  const unsigned lds0 = (unsigned)(size_t)smem;
  const int tid = threadIdx.x, lane = tid & 63;
  const int kq = __builtin_amdgcn_readfirstlane(tid >> 6);
  const int li = lane & 15, g = lane >> 4;
  const int idx = blockIdx.x, xcd = idx & 7, r = idx >> 3;
  const int nt = r & 7, bb = xcd * 4 + (r >> 3);
  const int blk = bb >> 4;
  const char* og = ws + (size_t)bb * 10 * 128 * 1024 + (size_t)kq * 32 * 1024;
  const char* wg = ws + WS_WO + ((size_t)(blk * 8 + nt) * 128 + kq * 32) * 4096;
  unsigned ovoff[10];
#pragma unroll
  for (int rb = 0; rb < 10; ++rb) ovoff[rb] = lane * 16 + rb * 131072;

  asm volatile("" ::: ACLOB2);
  s16x8 of[3][10], wf[3][4];
  auto ld = [&](auto SLC, auto IT, int ks) {
    constexpr int sl = decltype(SLC)::value, it = decltype(IT)::value;
    if constexpr (it < 4) wf[sl][it] = gload16<it * 1024>(ovoff[0], wg + (size_t)ks * 4096);
    else of[sl][it - 4] = gload16<0>(ovoff[it - 4], og + (size_t)ks * 1024);
  };
  sfor<14>([&](auto I) { ld(IC<0>{}, I, 0); });
  sfor<14>([&](auto I) { ld(IC<1>{}, I, 1); });
  sfor<40>([&](auto I) { acc_zero4<4 * decltype(I)::value>(); });
  auto step = [&](auto PHC, int ks) {
    constexpr int PH = decltype(PHC)::value;
    const int kn = ks + 2 < 32 ? ks + 2 : 31;
    WAITV(14);
    asm volatile("" : "+v"(of[PH][0]), "+v"(of[PH][1]), "+v"(of[PH][2]), "+v"(of[PH][3]), "+v"(of[PH][4]), "+v"(of[PH][5]), "+v"(of[PH][6]), "+v"(of[PH][7]), "+v"(of[PH][8]), "+v"(of[PH][9]));
    asm volatile("" : "+v"(wf[PH][0]), "+v"(wf[PH][1]), "+v"(wf[PH][2]), "+v"(wf[PH][3]));
    SB();
    sfor<40>([&](auto M) { constexpr int m = decltype(M)::value, nb = m / 10, rb = m % 10;
      mfma_acc<(rb * 4 + nb) * 4>(wf[PH][nb], of[PH][rb]);
      if constexpr ((m & 1) == 0 && m / 2 < 14) ld(IC<(PH + 2) % 3>{}, IC<m / 2>{}, kn);
      SB();
    });
  };
  for (int ks = 0; ks < 30; ks += 3) { step(IC<0>{}, ks); step(IC<1>{}, ks + 1); step(IC<2>{}, ks + 2); }
  step(IC<0>{}, 30); step(IC<1>{}, 31);
  asm volatile("s_waitcnt vmcnt(0)\n\ts_nop 7\n\ts_nop 7" ::: "memory");
  f32x4 mine[10];
  sfor<4>([&](auto NBc) {
    constexpr int nb = decltype(NBc)::value;
    sfor<10>([&](auto R) {
      constexpr int rb = decltype(R)::value;
      f32x4 v = {acc_rd<(rb * 4 + nb) * 4>(), acc_rd<(rb * 4 + nb) * 4 + 1>(), acc_rd<(rb * 4 + nb) * 4 + 2>(), acc_rd<(rb * 4 + nb) * 4 + 3>()};
      if (nb == kq) mine[rb] = v;
      else { const int sl = (kq - nb - 1) & 3; *(lds_f32x4_ptr)(lds0 + ((nb * 3 + sl) * 10 + rb) * 1024 + lane * 16) = v; }
    });
  });
  __syncthreads();
  char* outp = (char*)out + (size_t)(bb & 15) * SQ * 2048 + blk * 1024 + nt * 128 + kq * 32 + g * 8;
#pragma unroll
  for (int rb = 0; rb < 10; ++rb) {
    f32x4 sacc = mine[rb];
#pragma unroll
    for (int sl = 0; sl < 3; ++sl) sacc += *(lds_f32x4_ptr)(lds0 + ((kq * 3 + sl) * 10 + rb) * 1024 + lane * 16);
    uint2 o2; o2.x = cvtpk(sacc[0], sacc[1]); o2.y = cvtpk(sacc[2], sacc[3]);
    *(uint2*)(outp + (size_t)(16 * rb + li) * 2048) = o2;
  }
}

extern "C" void kernel_launch(void* const* d_in, const int* in_sizes, int n_in,
                              void* d_out, int out_size, void* d_ws, size_t ws_size,
                              hipStream_t stream) {
  const __hip_bfloat16* xA = (const __hip_bfloat16*)d_in[0];
  const __hip_bfloat16* kA = (const __hip_bfloat16*)d_in[1];
  const __hip_bfloat16* vA = (const __hip_bfloat16*)d_in[2];
  const __hip_bfloat16* wqA = (const __hip_bfloat16*)d_in[3];
  const __hip_bfloat16* woA = (const __hip_bfloat16*)d_in[4];
  const __hip_bfloat16* xB = (const __hip_bfloat16*)d_in[5];
  const __hip_bfloat16* kB = (const __hip_bfloat16*)d_in[6];
  const __hip_bfloat16* vB = (const __hip_bfloat16*)d_in[7];
  const __hip_bfloat16* wqB = (const __hip_bfloat16*)d_in[8];
  const __hip_bfloat16* woB = (const __hip_bfloat16*)d_in[9];
  __hip_bfloat16* ows = (__hip_bfloat16*)d_ws;
  attn_kernel<<<512, 512, 0, stream>>>(xA, kA, vA, wqA, xB, kB, vB, wqB, woA, woB, ows);
  oproj_kernel<<<256, 256, 0, stream>>>((const char*)d_ws, (__hip_bfloat16*)d_out);
}
```

```cpp
#include <hip/hip_runtime.h>
#include <hip/hip_bf16.h>
#include <utility>
#include <type_traits>

typedef __attribute__((ext_vector_type(8))) short s16x8;
typedef __attribute__((ext_vector_type(4))) short s16x4;
typedef __attribute__((ext_vector_type(4))) float f32x4;
typedef __attribute__((ext_vector_type(2))) float f32x2;
typedef __attribute__((ext_vector_type(2))) __bf16 bf16x2;
typedef __attribute__((ext_vector_type(4))) unsigned u32x4;
typedef __attribute__((address_space(3))) s16x4* lds_s16x4_ptr;
typedef __attribute__((address_space(3))) s16x8* lds_s16x8_ptr;
typedef __attribute__((address_space(3))) f32x4* lds_f32x4_ptr;

#define DI __device__ __forceinline__

constexpr int NB = 16, SQ = 160, DM = 512, NH = 32, SK = 1024, DH = 128;
constexpr int NS = 5;
constexpr int SLOT = 16384;
constexpr float QSCALE = 0.088388347648318440f * 1.4426950408889634f;
constexpr size_t WS_WO = (size_t)48 << 20;
constexpr float THR = 8.0f;

DI unsigned cvtpk(float lo, float hi) { f32x2 v = {lo, hi}; bf16x2 b = __builtin_convertvector(v, bf16x2); return __builtin_bit_cast(unsigned, b); }
DI s16x8 pack8(f32x4 a, f32x4 b) { u32x4 w = {cvtpk(a[0], a[1]), cvtpk(a[2], a[3]), cvtpk(b[0], b[1]), cvtpk(b[2], b[3])}; return __builtin_bit_cast(s16x8, w); }
DI float max3f(float a, float b, float c) { float r; asm("v_max3_f32 %0, %1, %2, %3" : "=v"(r) : "v"(a), "v"(b), "v"(c)); return r; }
DI float max2f(float a, float b) { float r; asm("v_max_f32_e32 %0, %1, %2" : "=v"(r) : "v"(a), "v"(b)); return r; }
DI f32x4 mfma16(s16x8 a, s16x8 b, f32x4 c) { return __builtin_amdgcn_mfma_f32_16x16x32_bf16(a, b, c, 0, 0, 0); }
DI s16x8 lds_b128(unsigned addr) { return *(lds_s16x8_ptr)(addr); }
DI s16x4 lds_tr(unsigned addr) { return __builtin_amdgcn_ds_read_tr16_b64_v4i16((lds_s16x4_ptr)(addr)); }
DI s16x8 cat8(s16x4 a, s16x4 b) { return __builtin_shufflevector(a, b, 0, 1, 2, 3, 4, 5, 6, 7); }

DI void dma4(unsigned ldsaddr, unsigned v0, unsigned v1, unsigned v2, unsigned v3, const void* gbase) {
  asm volatile("s_mov_b32 m0, %0\n\ts_nop 0\n\t"
               "global_load_lds_dwordx4 %1, %5 nt\n\t"
               "global_load_lds_dwordx4 %2, %5 offset:1024 nt\n\t"
               "global_load_lds_dwordx4 %3, %5 offset:2048 nt\n\t"
               "global_load_lds_dwordx4 %4, %5 offset:3072 nt"
               :: "s"(ldsaddr), "v"(v0), "v"(v1), "v"(v2), "v"(v3), "s"(gbase) : "memory");
}
DI void dma1(unsigned ldsaddr, unsigned v0, const void* gbase) {
  asm volatile("s_mov_b32 m0, %0\n\ts_nop 0\n\t"
               "global_load_lds_dwordx4 %1, %2"
               :: "s"(ldsaddr), "v"(v0), "s"(gbase) : "memory");
}
template <int OFF> DI s16x8 gload16(unsigned voff, const void* gbase) {
  s16x8 r; asm volatile("global_load_dwordx4 %0, %1, %2 offset:%3" : "=v"(r) : "v"(voff), "s"(gbase), "i"(OFF) : "memory"); return r;
}
template <int OFF> DI unsigned gload_u16(unsigned voff, const void* gbase) {
  unsigned r; asm volatile("global_load_ushort %0, %1, %2 offset:%3" : "=v"(r) : "v"(voff), "s"(gbase), "i"(OFF) : "memory"); return r;
}
#define WAITV(n) asm volatile("s_waitcnt vmcnt(" #n ")" ::: "memory")
#define BARRIER() asm volatile("s_barrier" ::: "memory")
#define PIN5(a) asm volatile("" : "+v"(a[0]), "+v"(a[1]), "+v"(a[2]), "+v"(a[3]), "+v"(a[4]))

#define ACLOB "a0","a1","a2","a3","a4","a5","a6","a7","a8","a9","a10","a11","a12","a13","a14","a15","a16","a17","a18","a19","a20","a21","a22","a23","a24","a25","a26","a27","a28","a29","a30","a31","a32","a33","a34","a35","a36","a37","a38","a39","a40","a41","a42","a43","a44","a45","a46","a47","a48","a49","a50","a51","a52","a53","a54","a55","a56","a57","a58","a59","a60","a61","a62","a63","a64","a65","a66","a67","a68","a69","a70","a71","a72","a73","a74","a75","a76","a77","a78","a79","a80","a81","a82","a83","a84","a85","a86","a87","a88","a89","a90","a91","a92","a93","a94","a95","a96","a97","a98","a99","a100","a101","a102","a103","a104","a105","a106","a107","a108","a109","a110","a111","a112","a113","a114","a115","a116","a117","a118","a119","a120","a121","a122","a123","a124","a125","a126","a127"
#define SB() __builtin_amdgcn_sched_barrier(0)
template <int... I, class F> DI void sfor_impl(std::integer_sequence<int, I...>, F&& f) { (f(std::integral_constant<int, I>{}), ...); }
template <int N, class F> DI void sfor(F&& f) { sfor_impl(std::make_integer_sequence<int, N>{}, f); }
template <int N> using IC = std::integral_constant<int, N>;
constexpr int AO(int rb, int db) { return (rb * 8 + db) * 4; }
constexpr int AQ(int f) { return 96 + f * 4; }
template <int A> DI void mfma_acc(s16x8 a, s16x8 b) {
  asm volatile("v_mfma_f32_16x16x32_bf16 a[%c2:%c3], %0, %1, a[%c2:%c3]" :: "v"(a), "v"(b), "i"(A), "i"(A + 3));
}
template <int A> DI void mfma_qk0(f32x4& S, s16x8 kf, const f32x4& nm) {
  asm volatile("v_mfma_f32_16x16x32_bf16 %0, %1, a[%c3:%c4], %2" : "=&v"(S) : "v"(kf), "v"(nm), "i"(A), "i"(A + 3));
}
template <int A> DI void mfma_qk(f32x4& S, s16x8 kf) {
  asm volatile("v_mfma_f32_16x16x32_bf16 %0, %1, a[%c2:%c3], %0" : "+v"(S) : "v"(kf), "i"(A), "i"(A + 3));
}
DI void mfma_v0(f32x4& D, s16x8 a, s16x8 b, const f32x4& c) { asm volatile("v_mfma_f32_16x16x32_bf16 %0, %1, %2, %3" : "=&v"(D) : "v"(a), "v"(b), "v"(c)); }
DI void mfma_v(f32x4& D, s16x8 a, s16x8 b) { asm volatile("v_mfma_f32_16x16x32_bf16 %0, %1, %2, %0" : "+v"(D) : "v"(a), "v"(b)); }
template <int A> DI float acc_rd() { float r; asm volatile("v_accvgpr_read_b32 %0, a[%c1]" : "=v"(r) : "i"(A)); return r; }
template <int A> DI void acc_wr(unsigned v) { asm volatile("v_accvgpr_write_b32 a[%c1], %0" :: "v"(v), "i"(A)); }
template <int A> DI void acc_zero4() { asm volatile("v_accvgpr_write_b32 a[%c0], 0\n\tv_accvgpr_write_b32 a[%c1], 0\n\tv_accvgpr_write_b32 a[%c2], 0\n\tv_accvgpr_write_b32 a[%c3], 0" :: "i"(A), "i"(A + 1), "i"(A + 2), "i"(A + 3)); }
template <int A> DI void acc_scale(float f) { float t; asm volatile("v_accvgpr_read_b32 %0, a[%c2]\n\tv_mul_f32 %0, %0, %1\n\tv_accvgpr_write_b32 a[%c2], %0" : "=&v"(t) : "v"(f), "i"(A)); }
DI void add4(float& l, const f32x4& v) { asm volatile("v_add_f32_e32 %0, %0, %1\n\tv_add_f32_e32 %0, %0, %2\n\tv_add_f32_e32 %0, %0, %3\n\tv_add_f32_e32 %0, %0, %4" : "+v"(l) : "v"(v[0]), "v"(v[1]), "v"(v[2]), "v"(v[3])); }
DI float fadd_s(float a, float b) { float r; asm("v_add_f32_e32 %0, %1, %2" : "=v"(r) : "v"(a), "v"(b)); return r; }
template <int LOFF> DI void dma4_nt(unsigned ldsw, unsigned v0, unsigned v1, unsigned v2, unsigned v3, const void* gbase) {
  asm volatile("s_add_u32 m0, %0, %6\n\ts_nop 0\n\t"
               "global_load_lds_dwordx4 %1, %5 nt\n\t"
               "global_load_lds_dwordx4 %2, %5 offset:1024 nt\n\t"
               "global_load_lds_dwordx4 %3, %5 offset:2048 nt\n\t"
               "global_load_lds_dwordx4 %4, %5 offset:3072 nt"
               :: "s"(ldsw), "v"(v0), "v"(v1), "v"(v2), "v"(v3), "s"(gbase), "i"(LOFF) : "memory", "scc");
}
template <int LOFF> DI void dma_m0(unsigned ldsw, unsigned v0, const void* gbase) {
  asm volatile("s_add_u32 m0, %0, %3\n\ts_nop 0\n\tglobal_load_lds_dwordx4 %1, %2" :: "s"(ldsw), "v"(v0), "s"(gbase), "i"(LOFF) : "memory", "scc");
}
#define TS(v) do { } while (0)
#define TS2(v) do { } while (0)
constexpr int TILE = 8192;
constexpr int XREG = 120 * 1024, XCH = 20480;

template <int NRB>
DI void attn_body(const unsigned short* wsrc, char* wdst, const char* xg, const char* wqg, const char* kg, const char* vg, char* ob, const unsigned lds0, const unsigned ldsh, const int r, const int bw, const int row0, const int lane
                  ) {
  constexpr bool SMALL = NRB == 2;
  const int li = lane & 15, g = lane >> 4, q4 = (lane >> 2) & 3, p4 = lane & 3;
  const unsigned ldsw = ldsh + r * 4096;
  unsigned voffK[4], voffV[2];
  const int hw = SMALL ? 2 + r : r;
#pragma unroll
  for (int n = 0; n < 4; ++n) voffK[n] = g * 256 + (((li ^ g) ^ (4 * n)) * 16);
#pragma unroll
  for (int n = 0; n < 2; ++n) voffV[n] = g * 256 + ((li ^ (g << 1) ^ (n << 3)) * 16);
  const unsigned voffW = g * 8192 + ((li ^ ((g & 1) | ((g >> 1) << 2) | (r << 3))) * 16);
  const unsigned ldsq = ldsh + hw * 2048;
  const char* wqw = wqg + (size_t)hw * 8 * 8192;
  unsigned wofs[4];
#pragma unroll
  for (int t = 0; t < 4; ++t) wofs[t] = ldsh + (8 * g + q4) * 256 + (((4 * t + p4) ^ ((q4 & 1) | ((q4 >> 1) << 2) | ((g & 1) << 3))) * 16) + (p4 & 1) * 8;
  unsigned voffX[2], xofs[2];
#pragma unroll
  for (int n = 0; n < 2; ++n) voffX[n] = (lane >> 3) * 1024 + (((lane & 7) ^ (4 * ((n + bw) & 1) + g)) * 16);
#pragma unroll
  for (int kk = 0; kk < 2; ++kk) xofs[kk] = lds0 + XREG + (row0 + li) * 128 + (((4 * kk + g) ^ (li >> 1)) * 16);

  unsigned long long tqa = 0, tqb = 0, sA = 0, sB = 0, sC = 0; (void)tqa; (void)tqb; (void)sA; (void)sB; (void)sC;
  unsigned long long ts0 = 0, ts1 = 0, ts3 = 0, ts4 = 0, tw0 = 0, tw1 = 0, twsum = 0, tqw = 0;
  (void)ts0; (void)ts1; (void)ts3; (void)ts4; (void)tw0; (void)tw1; (void)twsum; (void)tqw;
  TS(ts0);

  auto pieceW = [&](auto N, auto I) { constexpr int n = decltype(N)::value, i = decltype(I)::value;
    dma_m0<(n % 7) * TILE + i * 1024>(ldsq, voffW, wqw + (size_t)(32 * n) * 8192 + (size_t)i * 32768); };
  auto tileW = [&](auto N) { pieceW(N, IC<0>{}); pieceW(N, IC<1>{}); };
  auto tileK = [&](auto SL, const char* gb) { dma4_nt<decltype(SL)::value * TILE>(ldsw, voffK[0], voffK[1], voffK[2], voffK[3], gb); };
  auto tileV = [&](auto SL, const char* gb) { dma4_nt<decltype(SL)::value * TILE>(ldsw, voffV[0], voffV[1], voffV[0], voffV[1], gb); };

  const unsigned ldsx = lds0 + XREG + bw * 5120;
  const char* xgw = xg + (size_t)bw * 40 * 1024;
  auto pieceX = [&](auto C, auto PC) { constexpr int c = decltype(C)::value, pc = decltype(PC)::value;
    dma_m0<(c & 1) * XCH + pc * 1024>(ldsx, voffX[pc & 1], xgw + (size_t)pc * 8192 + c * 128); };
  auto chunkX = [&](auto C) { sfor<5>([&](auto PC) { pieceX(C, PC); }); };
  if constexpr (!SMALL) chunkX(IC<0>{});
  tileW(IC<0>{}); tileW(IC<1>{});
  if constexpr (!SMALL) chunkX(IC<1>{});
  tileW(IC<2>{}); tileW(IC<3>{}); tileW(IC<4>{}); tileW(IC<5>{}); tileW(IC<6>{});
  asm volatile("" ::: ACLOB);
  sfor<NRB * 8>([&](auto I) { acc_zero4<4 * decltype(I)::value>(); });
  s16x8 wf[2][8], xf[2][NRB];
  auto wread = [&](auto N, auto I) { constexpr int n = decltype(N)::value, i = decltype(I)::value;
    const unsigned a = (wofs[i >> 1] ^ ((i & 1) * 8)) + (n % 7) * TILE; wf[n & 1][i] = cat8(lds_tr(a), lds_tr(a + 1024)); };
  auto xread = [&](auto N, auto R) { constexpr int n = decltype(N)::value, rb = decltype(R)::value;
    xf[n & 1][rb] = lds_b128(xofs[n & 1] + ((n >> 1) & 1) * XCH + rb * 2048); };
  if constexpr (SMALL) WAITV(12); else WAITV(17);
  BARRIER();
  TS(tqa);
  sfor<NRB>([&](auto R) { xread(IC<0>{}, R); });
  sfor<8>([&](auto I) { wread(IC<0>{}, I); });
  auto qstep = [&](auto N, auto WSC) {
    constexpr int n = decltype(N)::value;
    constexpr int WN = SMALL ? decltype(WSC)::value : (n == 0 ? 15 : n == 1 ? 10 : n == 2 ? 15 : (n & 1) ? (n <= 9 ? 2 : 0) : 63);
    TS(tw0);
    asm volatile("s_waitcnt vmcnt(%0) lgkmcnt(0)" :: "i"(WN) : "memory");
    BARRIER();
    TS(tw1); tqw += tw1 - tw0;
    SB();
    constexpr bool hasW = n + 7 < 16, hasX = !SMALL && (n & 1) == 1 && (n + 3) / 2 < 8, hasKV = SMALL && n >= 9 && n <= 14;
    sfor<8 * NRB>([&](auto M) { constexpr int m = decltype(M)::value, d = m / NRB, rb = m % NRB;
      mfma_acc<AO(rb, d)>(wf[n & 1][d], xf[n & 1][rb]);
      if constexpr (n < 15) {
        if constexpr (m < NRB) xread(IC<n + 1>{}, IC<m>{});
        else if constexpr (m < NRB + 8) wread(IC<n + 1>{}, IC<m - NRB>{});
      }
      if constexpr (hasW && (m == 1 || m == 3)) pieceW(IC<hasW ? n + 7 : 0>{}, IC<m / 2>{});
      if constexpr (hasX && m >= 5 && m <= 13 && (m & 1)) pieceX(IC<hasX ? (n + 3) / 2 : 0>{}, IC<(m - 5) / 2>{});
      if constexpr (hasKV && m == 1) {
        constexpr int t = n - 9, sl = 2 * ((t / 2 + 1) % 4) + (t & 1);
        if constexpr ((t & 1) == 0) tileK(IC<sl>{}, kg + (size_t)(t / 2) * 8192); else tileV(IC<sl>{}, vg + (size_t)(t / 2) * 8192);
      }
      SB();
    });
  };
  qstep(IC<0>{}, IC<10>{}); qstep(IC<1>{}, IC<10>{}); qstep(IC<2>{}, IC<10>{}); qstep(IC<3>{}, IC<10>{});
  qstep(IC<4>{}, IC<10>{}); qstep(IC<5>{}, IC<10>{}); qstep(IC<6>{}, IC<10>{}); qstep(IC<7>{}, IC<10>{});
  qstep(IC<8>{}, IC<10>{}); qstep(IC<9>{}, IC<10>{}); qstep(IC<10>{}, IC<12>{}); qstep(IC<11>{}, IC<14>{});
  qstep(IC<12>{}, IC<16>{}); qstep(IC<13>{}, IC<18>{}); qstep(IC<14>{}, IC<20>{}); qstep(IC<15>{}, IC<63>{});
  TS(tqb);
  asm volatile("s_nop 7\n\ts_nop 7" ::: "memory");
  s16x8 Qv[4];
  sfor<NRB>([&](auto R) { sfor<4>([&](auto Tt) {
    constexpr int rb = decltype(R)::value, t = decltype(Tt)::value, f = rb * 4 + t;
    float v[8];
    sfor<8>([&](auto E) { v[decltype(E)::value] = acc_rd<AO(rb, 2 * t) + decltype(E)::value>() * QSCALE; });
    const unsigned c0 = cvtpk(v[0], v[1]), c1 = cvtpk(v[2], v[3]), c2 = cvtpk(v[4], v[5]), c3 = cvtpk(v[6], v[7]);
    const bool odd = (g & 1) != 0;
    const unsigned q0 = odd ? c2 : c0, q1 = odd ? c3 : c1, q2 = odd ? c0 : c2, q3 = odd ? c1 : c3;
    if constexpr (f < 8) { acc_wr<AQ(f)>(q0); acc_wr<AQ(f) + 1>(q1); acc_wr<AQ(f) + 2>(q2); acc_wr<AQ(f) + 3>(q3); }
    else { u32x4 q = {q0, q1, q2, q3}; Qv[f - 8] = __builtin_bit_cast(s16x8, q); }
  }); });
  sfor<NRB * 8>([&](auto I) { acc_zero4<4 * decltype(I)::value>(); });

  unsigned kofs[4], vofs[8];
  {
    int l2 = lane; asm volatile("" : "+v"(l2));
    const int li2 = l2 & 15, g2 = l2 >> 4, q2 = (l2 >> 2) & 3, p2 = l2 & 3;
#pragma unroll
    for (int ks = 0; ks < 4; ++ks) kofs[ks] = ldsh + li2 * 256 + (((4 * ks + g2) ^ li2) * 16);
    const unsigned r7 = 4 * (g2 & 1) + q2;
#pragma unroll
    for (int db = 0; db < 8; ++db) vofs[db] = ldsh + (4 * g2 + q2) * 256 + (r7 ^ db) * 32 + (p2 >> 1) * 16 + (p2 & 1) * 8;
  }
  f32x4 NM[NRB], S[NRB][2];
  float lsum[NRB];
  unsigned Pw[NRB][4];
#pragma unroll
  for (int rb = 0; rb < NRB; ++rb) { lsum[rb] = 0.f; NM[rb] = f32x4{0.f, 0.f, 0.f, 0.f}; }
  auto rowmax = [&](const f32x4 (&Sr)[2]) {
    float t = max3f(Sr[0][0], Sr[0][1], Sr[0][2]);
    t = max3f(t, Sr[0][3], Sr[1][0]); t = max3f(t, Sr[1][1], Sr[1][2]); t = max2f(t, Sr[1][3]);
    return t;
  };
  auto xlane_max = [&](float t) { t = fmaxf(t, __shfl_xor(t, 16)); t = fmaxf(t, __shfl_xor(t, 32)); return t; };
  auto sm_item = [&](int rb, int k) {
    if (k < 8) { float t = __builtin_amdgcn_exp2f(S[rb][k >> 2][k & 3]); asm volatile("" : "+v"(t)); S[rb][k >> 2][k & 3] = t; }
    else if (k < 10) add4(lsum[rb], S[rb][k - 8]);
    else { const int c = k - 10; unsigned t = cvtpk(S[rb][c >> 1][(c & 1) * 2], S[rb][c >> 1][(c & 1) * 2 + 1]); asm volatile("" : "+v"(t)); Pw[rb][c] = t; }
  };
  auto Pf = [&](int rb) { u32x4 v = {Pw[rb][0], Pw[rb][1], Pw[rb][2], Pw[rb][3]}; return __builtin_bit_cast(s16x8, v); };

  TS(ts1);
  s16x8 vf[8], kf[2][4];
  auto do_kread = [&](auto KSC) {
    constexpr int KS = decltype(KSC)::value;
#pragma unroll
    for (int m = 0; m < 8; ++m) kf[m >> 2][m & 3] = lds_b128(kofs[m & 3] + KS * TILE + (m >> 2) * 4096);
    SB();
  };
  auto do_qk = [&](auto VSC, auto VRC) {
    constexpr int VS = decltype(VSC)::value; constexpr bool VR = decltype(VRC)::value != 0;
    sfor<8 * NRB>([&](auto M) {
      constexpr int m = decltype(M)::value, g8 = m / NRB, rb = m % NRB, kb = g8 >> 2, ks = g8 & 3, f = rb * 4 + ks;
      if constexpr (f < 8) { if constexpr (ks == 0) mfma_qk0<AQ(f)>(S[rb][kb], kf[kb][ks], NM[rb]); else mfma_qk<AQ(f)>(S[rb][kb], kf[kb][ks]); }
      else { if constexpr (ks == 0) mfma_v0(S[rb][kb], kf[kb][ks], Qv[f - 8], NM[rb]); else mfma_v(S[rb][kb], kf[kb][ks], Qv[f - 8]); }
      if constexpr (VR && rb == NRB - 1) { const unsigned a = vofs[g8] + VS * TILE; vf[g8] = cat8(lds_tr(a), lds_tr(a + 4096)); }
      SB();
    });
    if constexpr (NRB == 3) asm volatile("s_nop 7" : "+v"(S[0][0]), "+v"(S[0][1]), "+v"(S[1][0]), "+v"(S[1][1]), "+v"(S[2][0]), "+v"(S[2][1]));
    else asm volatile("s_nop 7" : "+v"(S[0][0]), "+v"(S[0][1]), "+v"(S[1][0]), "+v"(S[1][1]));
  };
  auto do_check = [&](bool first) {
    float ta = max2f(S[0][0][0], S[0][0][1]), tb = max2f(S[0][0][2], S[0][0][3]);
#pragma unroll
    for (int i = 1; i < 2 * NRB; ++i) { ta = max3f(ta, S[i >> 1][i & 1][0], S[i >> 1][i & 1][1]); tb = max3f(tb, S[i >> 1][i & 1][2], S[i >> 1][i & 1][3]); }
    if (__builtin_expect(first || __any(max2f(ta, tb) > THR), 0)) {
      sfor<NRB>([&](auto R) {
        constexpr int rb = decltype(R)::value;
        const float rm = xlane_max(rowmax(S[rb]));
        const float dl = first ? rm : fmaxf(rm, 0.f);
        const float al = first ? 1.f : __builtin_amdgcn_exp2f(-dl);
        S[rb][0] -= dl; S[rb][1] -= dl; NM[rb] -= dl; lsum[rb] *= al;
        if (!first) sfor<32>([&](auto E) { acc_scale<AO(rb, 0) + decltype(E)::value>(al); });
      });
    }
  };
  auto do_smpv = [&](auto NPC) {
    constexpr int NP = decltype(NPC)::value;
#pragma unroll
    for (int k = 0; k < 14; ++k) sm_item(0, k);
    asm volatile("s_nop 1" ::: "memory");
    SB();
    sfor<8 * NP>([&](auto M) {
      constexpr int m = decltype(M)::value, rb = m / 8, db = m % 8;
      mfma_acc<AO(rb, db)>(vf[db], Pf(rb));
      if constexpr (rb + 1 < NRB) {
        constexpr int k0 = db < 4 ? 2 * db : db < 6 ? 4 + db : 10 + 2 * (db - 6), k1 = db < 4 ? 2 * db + 2 : db < 6 ? 5 + db : 12 + 2 * (db - 6);
#pragma unroll
        for (int k = k0; k < k1; ++k) sm_item(rb + 1, k);
        if constexpr (db == 7) asm volatile("s_nop 1" ::: "memory");
      }
      SB();
    });
  };
#pragma unroll
  for (int rb = 0; rb < NRB; ++rb) { S[rb][0] = f32x4{-1e30f, -1e30f, -1e30f, -1e30f}; S[rb][1] = S[rb][0]; Pw[rb][0] = Pw[rb][1] = Pw[rb][2] = Pw[rb][3] = 0u; }
#pragma unroll
  for (int db = 0; db < 8; ++db) { vf[db] = s16x8{0, 0, 0, 0, 0, 0, 0, 0}; kf[db >> 2][db & 3] = vf[db]; }
  auto wait_vf = [&]() { asm volatile("s_waitcnt lgkmcnt(0)" : "+v"(vf[0]), "+v"(vf[1]), "+v"(vf[2]), "+v"(vf[3]), "+v"(vf[4]), "+v"(vf[5]), "+v"(vf[6]), "+v"(vf[7]) :: "memory"); };
  auto aiter = [&](int j, auto PHC) {
    constexpr int PH = decltype(PHC)::value;
    constexpr int KS = 2 * ((PH + 1) % 4), VS = KS + 1, KD = 2 * PH, VD = KD + 1;
    TS(tw0);
    if constexpr (SMALL) { if (j < 30) WAITV(16); else WAITV(0); }
    BARRIER();
    TS(tw1); twsum += tw1 - tw0;
    do_kread(IC<KS>{});
    if constexpr (SMALL) {
      if (j + 3 < 32) tileK(IC<KD>{}, kg + (size_t)(j + 3) * 8192);
      SB();
      do_smpv(IC<NRB>{});
      TS2(tw0); sA += tw0 - tw1;
      do_qk(IC<VS>{}, IC<1>{});
      TS2(tw1); sB += tw1 - tw0;
      do_check(j == 0);
      TS2(tw0); sC += tw0 - tw1;
      if (j + 3 < 32) tileV(IC<VD>{}, vg + (size_t)(j + 3) * 8192);
      wait_vf();
    } else {
      do_qk(IC<VS>{}, IC<1>{});
      TS2(tw0); sA += tw0 - tw1;
      do_check(j == 0);
      TS2(tw1); sB += tw1 - tw0;
      do_smpv(IC<NRB>{});
      TS2(tw0); sC += tw0 - tw1;
    }
  };
  for (int j = 0; j < 32; j += 4) { aiter(j, IC<0>{}); aiter(j + 1, IC<1>{}); aiter(j + 2, IC<2>{}); aiter(j + 3, IC<3>{}); }
  unsigned rp[2][8];
  {
    const unsigned ro0 = (4 * g) * 1024 + li * 2, ro1 = ro0 + 16384;
    sfor<2>([&](auto F) { sfor<8>([&](auto J) {
      constexpr int f = decltype(F)::value, j = decltype(J)::value;
      rp[f][j] = gload_u16<(j & 3) * 1024 + f * 32>(j < 4 ? ro0 : ro1, wsrc);
    }); });
  }
  if constexpr (SMALL) do_smpv(IC<NRB>{});

  TS(ts3);
  asm volatile("s_nop 7\n\ts_nop 7" ::: "memory");
  sfor<NRB>([&](auto R) {
    constexpr int rb = decltype(R)::value;
    float l = lsum[rb]; l += __shfl_xor(l, 16); l += __shfl_xor(l, 32);
    const float il = __builtin_amdgcn_rcpf(l);
    sfor<4>([&](auto Tt) {
      constexpr int t = decltype(Tt)::value;
      float v[8];
      sfor<8>([&](auto E) { v[decltype(E)::value] = acc_rd<AO(rb, 2 * t) + decltype(E)::value>() * il; });
      u32x4 o = {cvtpk(v[0], v[1]), cvtpk(v[2], v[3]), cvtpk(v[4], v[5]), cvtpk(v[6], v[7])};
      *(u32x4*)(ob + (size_t)rb * (NH * 4 * 1024) + t * 1024) = o;
    });
  });
  asm volatile("s_waitcnt vmcnt(%c16)" : "+v"(rp[0][0]), "+v"(rp[0][1]), "+v"(rp[0][2]), "+v"(rp[0][3]), "+v"(rp[0][4]), "+v"(rp[0][5]), "+v"(rp[0][6]), "+v"(rp[0][7]),
               "+v"(rp[1][0]), "+v"(rp[1][1]), "+v"(rp[1][2]), "+v"(rp[1][3]), "+v"(rp[1][4]), "+v"(rp[1][5]), "+v"(rp[1][6]), "+v"(rp[1][7]) : "i"(NRB * 4) : "memory");
#pragma unroll
  for (int f = 0; f < 2; ++f) {
    u32x4 v = {rp[f][0] | (rp[f][1] << 16), rp[f][2] | (rp[f][3] << 16), rp[f][4] | (rp[f][5] << 16), rp[f][6] | (rp[f][7] << 16)};
    *(u32x4*)(wdst + f * 1024 + lane * 16) = v;
  }
}

__global__ __launch_bounds__(512, 2)
void attn_kernel(const __hip_bfloat16* __restrict__ xA, const __hip_bfloat16* __restrict__ kA, const __hip_bfloat16* __restrict__ vA, const __hip_bfloat16* __restrict__ wqA,
                 const __hip_bfloat16* __restrict__ xB, const __hip_bfloat16* __restrict__ kB, const __hip_bfloat16* __restrict__ vB, const __hip_bfloat16* __restrict__ wqB,
                 const __hip_bfloat16* __restrict__ woA, const __hip_bfloat16* __restrict__ woB, __hip_bfloat16* __restrict__ ows) {
  __shared__ __attribute__((aligned(16))) char smem[20 * TILE];
  const unsigned lds0 = (unsigned)(size_t)smem;
  const int tid = threadIdx.x, lane = tid & 63;
  const int w = __builtin_amdgcn_readfirstlane(tid >> 6);
  const int e = w & 1, r = (w >> 1) & 1;
  const int idx = blockIdx.x, xcd = idx & 7, j0 = idx >> 3, blk = j0 >> 5, jj = j0 & 31;
  const int b = (xcd >> 2) * 8 + (jj >> 2), h = ((xcd & 3) * 4 + (jj & 3)) * 2 + e;
  const char* xg = (const char*)(blk ? xB : xA) + (size_t)b * SQ * DM * 2;
  const char* wqg = (const char*)(blk ? wqB : wqA) + (size_t)h * DH * 2;
  const char* kg = (const char*)(blk ? kB : kA) + ((size_t)b * NH + h) * SK * DH * 2 + r * 4096;
  const char* vg = (const char*)(blk ? vB : vA) + ((size_t)b * NH + h) * SK * DH * 2 + r * 4096;
  const unsigned ldsh = lds0 + e * (8 * TILE);
  const int rbg = w < 4 ? 3 * r : 6 + 2 * r;
  char* ob = (char*)ows + ((((size_t)(blk * NB + b) * 10 + rbg) * NH + h) * 4) * 1024 + lane * 16;
  const int rpp = idx & 255, rnt = rpp & 7, rks = (rpp >> 3) * 4 + (w >> 1);
  const unsigned short* wsrc = (const unsigned short*)((idx >> 8) ? woB : woA) + (size_t)rks * 32 * DM + rnt * 64 + (w & 1) * 32;
  char* wdst = (char*)ows + WS_WO + ((size_t)((idx >> 8) * 8 + rnt) * 128 + rks) * 4096 + (w & 1) * 2048;
  if (w < 4) attn_body<3>(wsrc, wdst, xg, wqg, kg, vg, ob, lds0, ldsh, r, w & 3, 16 * rbg, lane); else attn_body<2>(wsrc, wdst, xg, wqg, kg, vg, ob, lds0, ldsh, r, w & 3, 16 * rbg, lane);
}

constexpr int K2_LDS = 160 * 1024;
__global__ __launch_bounds__(512, 2)
void oproj_kernel(const char* __restrict__ ws, __hip_bfloat16* __restrict__ out) {
  __shared__ __attribute__((aligned(16))) char smem[K2_LDS];
  const unsigned lds0 = (unsigned)(size_t)smem;
  const int tid = threadIdx.x, lane = tid & 63;
  const int w = __builtin_amdgcn_readfirstlane(tid >> 6);
  const int li = lane & 15, g = lane >> 4;
  const int idx = blockIdx.x, xcd = idx & 7, r = idx >> 3;
  const int nt = r & 7, bb = xcd * 4 + (r >> 3);
  const int blk = bb >> 4;
  const char* og = ws + (size_t)bb * 10 * 128 * 1024 + (size_t)w * 16 * 1024;
  const char* wg = ws + WS_WO + ((size_t)(blk * 8 + nt) * 128 + w * 16) * 4096;
  unsigned ovoff[10];
#pragma unroll
  for (int rb = 0; rb < 10; ++rb) ovoff[rb] = lane * 16 + rb * 131072;

  asm volatile("" ::: ACLOB);
  s16x8 of[10], wf[2][4];
  f32x4 av[2][4];
#pragma unroll
  for (int i = 0; i < 8; ++i) av[i >> 2][i & 3] = f32x4{0.f, 0.f, 0.f, 0.f};
  sfor<4>([&](auto I) { wf[0][decltype(I)::value] = gload16<decltype(I)::value * 1024>(ovoff[0], wg); });
  sfor<10>([&](auto R) { of[decltype(R)::value] = gload16<0>(ovoff[decltype(R)::value], og); });
  sfor<32>([&](auto I) { acc_zero4<4 * decltype(I)::value>(); });
  auto pin1 = [&](s16x8& v) { asm volatile("s_waitcnt vmcnt(13)" : "+v"(v) :: "memory"); };
  auto pin4 = [&](s16x8 (&v)[4]) { asm volatile("" : "+v"(v[0]), "+v"(v[1]), "+v"(v[2]), "+v"(v[3])); };
  auto step = [&](auto PHC, int ks) {
    constexpr int PH = decltype(PHC)::value;
    const int kn = ks + 1 < 16 ? ks + 1 : 15;
    const char* wgn = wg + (size_t)kn * 4096; const char* ogn = og + (size_t)kn * 1024;
    sfor<4>([&](auto I) { wf[PH ^ 1][decltype(I)::value] = gload16<decltype(I)::value * 1024>(ovoff[0], wgn); });
    sfor<10>([&](auto R) {
      constexpr int rb = decltype(R)::value;
      pin1(of[rb]);
      if constexpr (rb == 0) pin4(wf[PH]);
      SB();
      sfor<4>([&](auto NBc) { constexpr int nb = decltype(NBc)::value;
        if constexpr (rb < 8) mfma_acc<(rb * 4 + nb) * 4>(wf[PH][nb], of[rb]); else mfma_v(av[rb - 8][nb], wf[PH][nb], of[rb]); });
      of[rb] = gload16<0>(ovoff[rb], ogn);
      SB();
    });
  };
  for (int ks = 0; ks < 16; ks += 2) { step(IC<0>{}, ks); step(IC<1>{}, ks + 1); }
  asm volatile("s_waitcnt vmcnt(0)\n\ts_nop 7\n\ts_nop 7" : "+v"(av[0][0]), "+v"(av[0][1]), "+v"(av[0][2]), "+v"(av[0][3]), "+v"(av[1][0]), "+v"(av[1][1]), "+v"(av[1][2]), "+v"(av[1][3]) :: "memory");
  auto getacc = [&](auto RC, auto NC) { constexpr int rb = decltype(RC)::value, nb = decltype(NC)::value;
    if constexpr (rb < 8) return f32x4{acc_rd<(rb * 4 + nb) * 4>(), acc_rd<(rb * 4 + nb) * 4 + 1>(), acc_rd<(rb * 4 + nb) * 4 + 2>(), acc_rd<(rb * 4 + nb) * 4 + 3>()};
    else return av[rb - 8][nb]; };
  char* outb = (char*)out + (size_t)(bb & 15) * SQ * 2048 + blk * 1024 + nt * 128 + g * 8;
  sfor<2>([&](auto HC) {
    constexpr int hf = decltype(HC)::value;
    if constexpr (hf == 1) __syncthreads();
    sfor<20>([&](auto F) { constexpr int f = decltype(F)::value;
      *(lds_f32x4_ptr)(lds0 + (w * 20 + f) * 1024 + lane * 16) = getacc(IC<hf * 5 + f / 4>{}, IC<f % 4>{}); });
    __syncthreads();
#pragma unroll
    for (int i = 0; i < 3; ++i) {
      const int f = w + 8 * i;
      if (f < 20) {
        f32x4 sacc = *(lds_f32x4_ptr)(lds0 + f * 1024 + lane * 16);
#pragma unroll
        for (int sw = 1; sw < 8; ++sw) sacc += *(lds_f32x4_ptr)(lds0 + (sw * 20 + f) * 1024 + lane * 16);
        uint2 o2; o2.x = cvtpk(sacc[0], sacc[1]); o2.y = cvtpk(sacc[2], sacc[3]);
        *(uint2*)(outb + (size_t)(16 * (hf * 5 + (f >> 2)) + li) * 2048 + (f & 3) * 32) = o2;
      }
    }
  });
}

extern "C" void kernel_launch(void* const* d_in, const int* in_sizes, int n_in,
                              void* d_out, int out_size, void* d_ws, size_t ws_size,
                              hipStream_t stream) {
  const __hip_bfloat16* xA = (const __hip_bfloat16*)d_in[0];
  const __hip_bfloat16* kA = (const __hip_bfloat16*)d_in[1];
  const __hip_bfloat16* vA = (const __hip_bfloat16*)d_in[2];
  const __hip_bfloat16* wqA = (const __hip_bfloat16*)d_in[3];
  const __hip_bfloat16* woA = (const __hip_bfloat16*)d_in[4];
  const __hip_bfloat16* xB = (const __hip_bfloat16*)d_in[5];
  const __hip_bfloat16* kB = (const __hip_bfloat16*)d_in[6];
  const __hip_bfloat16* vB = (const __hip_bfloat16*)d_in[7];
  const __hip_bfloat16* wqB = (const __hip_bfloat16*)d_in[8];
  const __hip_bfloat16* woB = (const __hip_bfloat16*)d_in[9];
  __hip_bfloat16* ows = (__hip_bfloat16*)d_ws;
  attn_kernel<<<512, 512, 0, stream>>>(xA, kA, vA, wqA, xB, kB, vB, wqB, woA, woB, ows);
  oproj_kernel<<<256, 512, 0, stream>>>((const char*)d_ws, (__hip_bfloat16*)d_out);
}
```

```cpp
#include <hip/hip_runtime.h>
#include <hip/hip_bf16.h>
#include <utility>
#include <type_traits>

typedef __attribute__((ext_vector_type(8))) short s16x8;
typedef __attribute__((ext_vector_type(4))) short s16x4;
typedef __attribute__((ext_vector_type(4))) float f32x4;
typedef __attribute__((ext_vector_type(2))) float f32x2;
typedef __attribute__((ext_vector_type(2))) __bf16 bf16x2;
typedef __attribute__((ext_vector_type(4))) unsigned u32x4;
typedef __attribute__((address_space(3))) s16x4* lds_s16x4_ptr;
typedef __attribute__((address_space(3))) s16x8* lds_s16x8_ptr;
typedef __attribute__((address_space(3))) f32x4* lds_f32x4_ptr;

#define DI __device__ __forceinline__

constexpr int NB = 16, SQ = 160, DM = 512, NH = 32, SK = 1024, DH = 128;
constexpr int NS = 5;
constexpr int SLOT = 16384;
constexpr float QSCALE = 0.088388347648318440f * 1.4426950408889634f;
constexpr size_t WS_WO = (size_t)48 << 20;
constexpr size_t WS_XCH = (size_t)96 << 20, WS_FLAG = (size_t)112 << 20;
constexpr float THR = 8.0f;

DI unsigned cvtpk(float lo, float hi) { f32x2 v = {lo, hi}; bf16x2 b = __builtin_convertvector(v, bf16x2); return __builtin_bit_cast(unsigned, b); }
DI s16x8 pack8(f32x4 a, f32x4 b) { u32x4 w = {cvtpk(a[0], a[1]), cvtpk(a[2], a[3]), cvtpk(b[0], b[1]), cvtpk(b[2], b[3])}; return __builtin_bit_cast(s16x8, w); }
DI float max3f(float a, float b, float c) { float r; asm("v_max3_f32 %0, %1, %2, %3" : "=v"(r) : "v"(a), "v"(b), "v"(c)); return r; }
DI float max2f(float a, float b) { float r; asm("v_max_f32_e32 %0, %1, %2" : "=v"(r) : "v"(a), "v"(b)); return r; }
DI f32x4 mfma16(s16x8 a, s16x8 b, f32x4 c) { return __builtin_amdgcn_mfma_f32_16x16x32_bf16(a, b, c, 0, 0, 0); }
DI s16x8 lds_b128(unsigned addr) { return *(lds_s16x8_ptr)(addr); }
DI s16x4 lds_tr(unsigned addr) { return __builtin_amdgcn_ds_read_tr16_b64_v4i16((lds_s16x4_ptr)(addr)); }
DI s16x8 cat8(s16x4 a, s16x4 b) { return __builtin_shufflevector(a, b, 0, 1, 2, 3, 4, 5, 6, 7); }

DI void dma4(unsigned ldsaddr, unsigned v0, unsigned v1, unsigned v2, unsigned v3, const void* gbase) {
  asm volatile("s_mov_b32 m0, %0\n\ts_nop 0\n\t"
               "global_load_lds_dwordx4 %1, %5 nt\n\t"
               "global_load_lds_dwordx4 %2, %5 offset:1024 nt\n\t"
               "global_load_lds_dwordx4 %3, %5 offset:2048 nt\n\t"
               "global_load_lds_dwordx4 %4, %5 offset:3072 nt"
               :: "s"(ldsaddr), "v"(v0), "v"(v1), "v"(v2), "v"(v3), "s"(gbase) : "memory");
}
DI void dma1(unsigned ldsaddr, unsigned v0, const void* gbase) {
  asm volatile("s_mov_b32 m0, %0\n\ts_nop 0\n\t"
               "global_load_lds_dwordx4 %1, %2"
               :: "s"(ldsaddr), "v"(v0), "s"(gbase) : "memory");
}
template <int OFF> DI s16x8 gload16(unsigned voff, const void* gbase) {
  s16x8 r; asm volatile("global_load_dwordx4 %0, %1, %2 offset:%3" : "=v"(r) : "v"(voff), "s"(gbase), "i"(OFF) : "memory"); return r;
}
template <int OFF> DI unsigned gload_u16(unsigned voff, const void* gbase) {
  unsigned r; asm volatile("global_load_ushort %0, %1, %2 offset:%3" : "=v"(r) : "v"(voff), "s"(gbase), "i"(OFF) : "memory"); return r;
}
#define WAITV(n) asm volatile("s_waitcnt vmcnt(" #n ")" ::: "memory")
#define BARRIER() asm volatile("s_barrier" ::: "memory")
#define PIN5(a) asm volatile("" : "+v"(a[0]), "+v"(a[1]), "+v"(a[2]), "+v"(a[3]), "+v"(a[4]))

#define ACLOB "a0","a1","a2","a3","a4","a5","a6","a7","a8","a9","a10","a11","a12","a13","a14","a15","a16","a17","a18","a19","a20","a21","a22","a23","a24","a25","a26","a27","a28","a29","a30","a31","a32","a33","a34","a35","a36","a37","a38","a39","a40","a41","a42","a43","a44","a45","a46","a47","a48","a49","a50","a51","a52","a53","a54","a55","a56","a57","a58","a59","a60","a61","a62","a63","a64","a65","a66","a67","a68","a69","a70","a71","a72","a73","a74","a75","a76","a77","a78","a79","a80","a81","a82","a83","a84","a85","a86","a87","a88","a89","a90","a91","a92","a93","a94","a95","a96","a97","a98","a99","a100","a101","a102","a103","a104","a105","a106","a107","a108","a109","a110","a111","a112","a113","a114","a115","a116","a117","a118","a119","a120","a121","a122","a123","a124","a125","a126","a127"
#define SB() __builtin_amdgcn_sched_barrier(0)
template <int... I, class F> DI void sfor_impl(std::integer_sequence<int, I...>, F&& f) { (f(std::integral_constant<int, I>{}), ...); }
template <int N, class F> DI void sfor(F&& f) { sfor_impl(std::make_integer_sequence<int, N>{}, f); }
template <int N> using IC = std::integral_constant<int, N>;
constexpr int AO(int rb, int db) { return (rb * 8 + db) * 4; }
constexpr int AQ(int f) { return 96 + f * 4; }
template <int A> DI void mfma_acc(s16x8 a, s16x8 b) {
  asm volatile("v_mfma_f32_16x16x32_bf16 a[%c2:%c3], %0, %1, a[%c2:%c3]" :: "v"(a), "v"(b), "i"(A), "i"(A + 3));
}
template <int A> DI void mfma_qk0(f32x4& S, s16x8 kf, const f32x4& nm) {
  asm volatile("v_mfma_f32_16x16x32_bf16 %0, %1, a[%c3:%c4], %2" : "=&v"(S) : "v"(kf), "v"(nm), "i"(A), "i"(A + 3));
}
template <int A> DI void mfma_qk(f32x4& S, s16x8 kf) {
  asm volatile("v_mfma_f32_16x16x32_bf16 %0, %1, a[%c2:%c3], %0" : "+v"(S) : "v"(kf), "i"(A), "i"(A + 3));
}
DI void mfma_v0(f32x4& D, s16x8 a, s16x8 b, const f32x4& c) { asm volatile("v_mfma_f32_16x16x32_bf16 %0, %1, %2, %3" : "=&v"(D) : "v"(a), "v"(b), "v"(c)); }
DI void mfma_v(f32x4& D, s16x8 a, s16x8 b) { asm volatile("v_mfma_f32_16x16x32_bf16 %0, %1, %2, %0" : "+v"(D) : "v"(a), "v"(b)); }
template <int A> DI float acc_rd() { float r; asm volatile("v_accvgpr_read_b32 %0, a[%c1]" : "=v"(r) : "i"(A)); return r; }
template <int A> DI void acc_wr(unsigned v) { asm volatile("v_accvgpr_write_b32 a[%c1], %0" :: "v"(v), "i"(A)); }
template <int A> DI void acc_zero4() { asm volatile("v_accvgpr_write_b32 a[%c0], 0\n\tv_accvgpr_write_b32 a[%c1], 0\n\tv_accvgpr_write_b32 a[%c2], 0\n\tv_accvgpr_write_b32 a[%c3], 0" :: "i"(A), "i"(A + 1), "i"(A + 2), "i"(A + 3)); }
template <int A> DI void acc_scale(float f) { float t; asm volatile("v_accvgpr_read_b32 %0, a[%c2]\n\tv_mul_f32 %0, %0, %1\n\tv_accvgpr_write_b32 a[%c2], %0" : "=&v"(t) : "v"(f), "i"(A)); }
DI void add4(float& l, const f32x4& v) { asm volatile("v_add_f32_e32 %0, %0, %1\n\tv_add_f32_e32 %0, %0, %2\n\tv_add_f32_e32 %0, %0, %3\n\tv_add_f32_e32 %0, %0, %4" : "+v"(l) : "v"(v[0]), "v"(v[1]), "v"(v[2]), "v"(v[3])); }
DI float fadd_s(float a, float b) { float r; asm("v_add_f32_e32 %0, %1, %2" : "=v"(r) : "v"(a), "v"(b)); return r; }
template <int LOFF> DI void dma4_nt(unsigned ldsw, unsigned v0, unsigned v1, unsigned v2, unsigned v3, const void* gbase) {
  asm volatile("s_add_u32 m0, %0, %6\n\ts_nop 0\n\t"
               "global_load_lds_dwordx4 %1, %5 nt\n\t"
               "global_load_lds_dwordx4 %2, %5 offset:1024 nt\n\t"
               "global_load_lds_dwordx4 %3, %5 offset:2048 nt\n\t"
               "global_load_lds_dwordx4 %4, %5 offset:3072 nt"
               :: "s"(ldsw), "v"(v0), "v"(v1), "v"(v2), "v"(v3), "s"(gbase), "i"(LOFF) : "memory", "scc");
}
template <int LOFF> DI void dma_m0(unsigned ldsw, unsigned v0, const void* gbase) {
  asm volatile("s_add_u32 m0, %0, %3\n\ts_nop 0\n\tglobal_load_lds_dwordx4 %1, %2" :: "s"(ldsw), "v"(v0), "s"(gbase), "i"(LOFF) : "memory", "scc");
}
#define TS(v) do { } while (0)
#define TS2(v) do { } while (0)
constexpr int TILE = 8192;
constexpr int XREG = 120 * 1024, XCH = 20480;

template <int NRB>
DI void attn_body(const unsigned short* wsrc, char* wdst, const char* xg, const char* wqg, const char* kg, const char* vg, char* ob, const unsigned lds0, const unsigned ldsh, const int r, const int bw, const int row0, const int lane
                  ) {
  constexpr bool SMALL = NRB == 2;
  const int li = lane & 15, g = lane >> 4, q4 = (lane >> 2) & 3, p4 = lane & 3;
  const unsigned ldsw = ldsh + r * 4096;
  unsigned voffK[4], voffV[2];
  const int hw = SMALL ? 2 + r : r;
#pragma unroll
  for (int n = 0; n < 4; ++n) voffK[n] = g * 256 + (((li ^ g) ^ (4 * n)) * 16);
#pragma unroll
  for (int n = 0; n < 2; ++n) voffV[n] = g * 256 + ((li ^ (g << 1) ^ (n << 3)) * 16);
  const unsigned voffW = g * 8192 + ((li ^ ((g & 1) | ((g >> 1) << 2) | (r << 3))) * 16);
  const unsigned ldsq = ldsh + hw * 2048;
  const char* wqw = wqg + (size_t)hw * 8 * 8192;
  unsigned wofs[4];
#pragma unroll
  for (int t = 0; t < 4; ++t) wofs[t] = ldsh + (8 * g + q4) * 256 + (((4 * t + p4) ^ ((q4 & 1) | ((q4 >> 1) << 2) | ((g & 1) << 3))) * 16) + (p4 & 1) * 8;
  unsigned voffX[2], xofs[2];
#pragma unroll
  for (int n = 0; n < 2; ++n) voffX[n] = (lane >> 3) * 1024 + (((lane & 7) ^ (4 * ((n + bw) & 1) + g)) * 16);
#pragma unroll
  for (int kk = 0; kk < 2; ++kk) xofs[kk] = lds0 + XREG + (row0 + li) * 128 + (((4 * kk + g) ^ (li >> 1)) * 16);

  unsigned long long tqa = 0, tqb = 0, sA = 0, sB = 0, sC = 0; (void)tqa; (void)tqb; (void)sA; (void)sB; (void)sC;
  unsigned long long ts0 = 0, ts1 = 0, ts3 = 0, ts4 = 0, tw0 = 0, tw1 = 0, twsum = 0, tqw = 0;
  (void)ts0; (void)ts1; (void)ts3; (void)ts4; (void)tw0; (void)tw1; (void)twsum; (void)tqw;
  TS(ts0);

  auto pieceW = [&](auto N, auto I) { constexpr int n = decltype(N)::value, i = decltype(I)::value;
    dma_m0<(n % 7) * TILE + i * 1024>(ldsq, voffW, wqw + (size_t)(32 * n) * 8192 + (size_t)i * 32768); };
  auto tileW = [&](auto N) { pieceW(N, IC<0>{}); pieceW(N, IC<1>{}); };
  auto tileK = [&](auto SL, const char* gb) { dma4_nt<decltype(SL)::value * TILE>(ldsw, voffK[0], voffK[1], voffK[2], voffK[3], gb); };
  auto tileV = [&](auto SL, const char* gb) { dma4_nt<decltype(SL)::value * TILE>(ldsw, voffV[0], voffV[1], voffV[0], voffV[1], gb); };

  const unsigned ldsx = lds0 + XREG + bw * 5120;
  const char* xgw = xg + (size_t)bw * 40 * 1024;
  auto pieceX = [&](auto C, auto PC) { constexpr int c = decltype(C)::value, pc = decltype(PC)::value;
    dma_m0<(c & 1) * XCH + pc * 1024>(ldsx, voffX[pc & 1], xgw + (size_t)pc * 8192 + c * 128); };
  auto chunkX = [&](auto C) { sfor<5>([&](auto PC) { pieceX(C, PC); }); };
  if constexpr (!SMALL) chunkX(IC<0>{});
  tileW(IC<0>{}); tileW(IC<1>{});
  if constexpr (!SMALL) chunkX(IC<1>{});
  tileW(IC<2>{}); tileW(IC<3>{}); tileW(IC<4>{}); tileW(IC<5>{}); tileW(IC<6>{});
  asm volatile("" ::: ACLOB);
  sfor<NRB * 8>([&](auto I) { acc_zero4<4 * decltype(I)::value>(); });
  s16x8 wf[2][8], xf[2][NRB];
  auto wread = [&](auto N, auto I) { constexpr int n = decltype(N)::value, i = decltype(I)::value;
    const unsigned a = (wofs[i >> 1] ^ ((i & 1) * 8)) + (n % 7) * TILE; wf[n & 1][i] = cat8(lds_tr(a), lds_tr(a + 1024)); };
  auto xread = [&](auto N, auto R) { constexpr int n = decltype(N)::value, rb = decltype(R)::value;
    xf[n & 1][rb] = lds_b128(xofs[n & 1] + ((n >> 1) & 1) * XCH + rb * 2048); };
  if constexpr (SMALL) WAITV(12); else WAITV(17);
  BARRIER();
  TS(tqa);
  sfor<NRB>([&](auto R) { xread(IC<0>{}, R); });
  sfor<8>([&](auto I) { wread(IC<0>{}, I); });
  auto qstep = [&](auto N, auto WSC) {
    constexpr int n = decltype(N)::value;
    constexpr int WN = SMALL ? decltype(WSC)::value : (n == 0 ? 15 : n == 1 ? 10 : n == 2 ? 15 : (n & 1) ? (n <= 9 ? 2 : 0) : 63);
    TS(tw0);
    asm volatile("s_waitcnt vmcnt(%0) lgkmcnt(0)" :: "i"(WN) : "memory");
    BARRIER();
    TS(tw1); tqw += tw1 - tw0;
    SB();
    constexpr bool hasW = n + 7 < 16, hasX = !SMALL && (n & 1) == 1 && (n + 3) / 2 < 8, hasKV = SMALL && n >= 9 && n <= 14;
    sfor<8 * NRB>([&](auto M) { constexpr int m = decltype(M)::value, d = m / NRB, rb = m % NRB;
      mfma_acc<AO(rb, d)>(wf[n & 1][d], xf[n & 1][rb]);
      if constexpr (n < 15) {
        if constexpr (m < NRB) xread(IC<n + 1>{}, IC<m>{});
        else if constexpr (m < NRB + 8) wread(IC<n + 1>{}, IC<m - NRB>{});
      }
      if constexpr (hasW && (m == 1 || m == 3)) pieceW(IC<hasW ? n + 7 : 0>{}, IC<m / 2>{});
      if constexpr (hasX && m >= 5 && m <= 13 && (m & 1)) pieceX(IC<hasX ? (n + 3) / 2 : 0>{}, IC<(m - 5) / 2>{});
      if constexpr (hasKV && m == 1) {
        constexpr int t = n - 9, sl = 2 * ((t / 2 + 1) % 4) + (t & 1);
        if constexpr ((t & 1) == 0) tileK(IC<sl>{}, kg + (size_t)(t / 2) * 8192); else tileV(IC<sl>{}, vg + (size_t)(t / 2) * 8192);
      }
      SB();
    });
  };
  qstep(IC<0>{}, IC<10>{}); qstep(IC<1>{}, IC<10>{}); qstep(IC<2>{}, IC<10>{}); qstep(IC<3>{}, IC<10>{});
  qstep(IC<4>{}, IC<10>{}); qstep(IC<5>{}, IC<10>{}); qstep(IC<6>{}, IC<10>{}); qstep(IC<7>{}, IC<10>{});
  qstep(IC<8>{}, IC<10>{}); qstep(IC<9>{}, IC<10>{}); qstep(IC<10>{}, IC<12>{}); qstep(IC<11>{}, IC<14>{});
  qstep(IC<12>{}, IC<16>{}); qstep(IC<13>{}, IC<18>{}); qstep(IC<14>{}, IC<20>{}); qstep(IC<15>{}, IC<63>{});
  TS(tqb);
  asm volatile("s_nop 7\n\ts_nop 7" ::: "memory");
  s16x8 Qv[4];
  sfor<NRB>([&](auto R) { sfor<4>([&](auto Tt) {
    constexpr int rb = decltype(R)::value, t = decltype(Tt)::value, f = rb * 4 + t;
    float v[8];
    sfor<8>([&](auto E) { v[decltype(E)::value] = acc_rd<AO(rb, 2 * t) + decltype(E)::value>() * QSCALE; });
    const unsigned c0 = cvtpk(v[0], v[1]), c1 = cvtpk(v[2], v[3]), c2 = cvtpk(v[4], v[5]), c3 = cvtpk(v[6], v[7]);
    const bool odd = (g & 1) != 0;
    const unsigned q0 = odd ? c2 : c0, q1 = odd ? c3 : c1, q2 = odd ? c0 : c2, q3 = odd ? c1 : c3;
    if constexpr (f < 8) { acc_wr<AQ(f)>(q0); acc_wr<AQ(f) + 1>(q1); acc_wr<AQ(f) + 2>(q2); acc_wr<AQ(f) + 3>(q3); }
    else { u32x4 q = {q0, q1, q2, q3}; Qv[f - 8] = __builtin_bit_cast(s16x8, q); }
  }); });
  sfor<NRB * 8>([&](auto I) { acc_zero4<4 * decltype(I)::value>(); });

  unsigned kofs[4], vofs[8];
  {
    int l2 = lane; asm volatile("" : "+v"(l2));
    const int li2 = l2 & 15, g2 = l2 >> 4, q2 = (l2 >> 2) & 3, p2 = l2 & 3;
#pragma unroll
    for (int ks = 0; ks < 4; ++ks) kofs[ks] = ldsh + li2 * 256 + (((4 * ks + g2) ^ li2) * 16);
    const unsigned r7 = 4 * (g2 & 1) + q2;
#pragma unroll
    for (int db = 0; db < 8; ++db) vofs[db] = ldsh + (4 * g2 + q2) * 256 + (r7 ^ db) * 32 + (p2 >> 1) * 16 + (p2 & 1) * 8;
  }
  f32x4 NM[NRB], S[NRB][2];
  float lsum[NRB];
  unsigned Pw[NRB][4];
#pragma unroll
  for (int rb = 0; rb < NRB; ++rb) { lsum[rb] = 0.f; NM[rb] = f32x4{0.f, 0.f, 0.f, 0.f}; }
  auto rowmax = [&](const f32x4 (&Sr)[2]) {
    float t = max3f(Sr[0][0], Sr[0][1], Sr[0][2]);
    t = max3f(t, Sr[0][3], Sr[1][0]); t = max3f(t, Sr[1][1], Sr[1][2]); t = max2f(t, Sr[1][3]);
    return t;
  };
  auto xlane_max = [&](float t) { t = fmaxf(t, __shfl_xor(t, 16)); t = fmaxf(t, __shfl_xor(t, 32)); return t; };
  auto sm_item = [&](int rb, int k) {
    if (k < 8) { float t = __builtin_amdgcn_exp2f(S[rb][k >> 2][k & 3]); asm volatile("" : "+v"(t)); S[rb][k >> 2][k & 3] = t; }
    else if (k < 10) add4(lsum[rb], S[rb][k - 8]);
    else { const int c = k - 10; unsigned t = cvtpk(S[rb][c >> 1][(c & 1) * 2], S[rb][c >> 1][(c & 1) * 2 + 1]); asm volatile("" : "+v"(t)); Pw[rb][c] = t; }
  };
  auto Pf = [&](int rb) { u32x4 v = {Pw[rb][0], Pw[rb][1], Pw[rb][2], Pw[rb][3]}; return __builtin_bit_cast(s16x8, v); };

  TS(ts1);
  s16x8 vf[8], kf[2][4];
  auto do_kread = [&](auto KSC) {
    constexpr int KS = decltype(KSC)::value;
#pragma unroll
    for (int m = 0; m < 8; ++m) kf[m >> 2][m & 3] = lds_b128(kofs[m & 3] + KS * TILE + (m >> 2) * 4096);
    SB();
  };
  auto do_qk = [&](auto VSC, auto VRC) {
    constexpr int VS = decltype(VSC)::value; constexpr bool VR = decltype(VRC)::value != 0;
    sfor<8 * NRB>([&](auto M) {
      constexpr int m = decltype(M)::value, g8 = m / NRB, rb = m % NRB, kb = g8 >> 2, ks = g8 & 3, f = rb * 4 + ks;
      if constexpr (f < 8) { if constexpr (ks == 0) mfma_qk0<AQ(f)>(S[rb][kb], kf[kb][ks], NM[rb]); else mfma_qk<AQ(f)>(S[rb][kb], kf[kb][ks]); }
      else { if constexpr (ks == 0) mfma_v0(S[rb][kb], kf[kb][ks], Qv[f - 8], NM[rb]); else mfma_v(S[rb][kb], kf[kb][ks], Qv[f - 8]); }
      if constexpr (VR && rb == NRB - 1) { const unsigned a = vofs[g8] + VS * TILE; vf[g8] = cat8(lds_tr(a), lds_tr(a + 4096)); }
      SB();
    });
    if constexpr (NRB == 3) asm volatile("s_nop 7" : "+v"(S[0][0]), "+v"(S[0][1]), "+v"(S[1][0]), "+v"(S[1][1]), "+v"(S[2][0]), "+v"(S[2][1]));
    else asm volatile("s_nop 7" : "+v"(S[0][0]), "+v"(S[0][1]), "+v"(S[1][0]), "+v"(S[1][1]));
  };
  auto do_check = [&](bool first) {
    float ta = max2f(S[0][0][0], S[0][0][1]), tb = max2f(S[0][0][2], S[0][0][3]);
#pragma unroll
    for (int i = 1; i < 2 * NRB; ++i) { ta = max3f(ta, S[i >> 1][i & 1][0], S[i >> 1][i & 1][1]); tb = max3f(tb, S[i >> 1][i & 1][2], S[i >> 1][i & 1][3]); }
    if (__builtin_expect(first || __any(max2f(ta, tb) > THR), 0)) {
      sfor<NRB>([&](auto R) {
        constexpr int rb = decltype(R)::value;
        const float rm = xlane_max(rowmax(S[rb]));
        const float dl = first ? rm : fmaxf(rm, 0.f);
        const float al = first ? 1.f : __builtin_amdgcn_exp2f(-dl);
        S[rb][0] -= dl; S[rb][1] -= dl; NM[rb] -= dl; lsum[rb] *= al;
        if (!first) sfor<32>([&](auto E) { acc_scale<AO(rb, 0) + decltype(E)::value>(al); });
      });
    }
  };
  auto do_smpv = [&](auto NPC) {
    constexpr int NP = decltype(NPC)::value;
#pragma unroll
    for (int k = 0; k < 14; ++k) sm_item(0, k);
    asm volatile("s_nop 1" ::: "memory");
    SB();
    sfor<8 * NP>([&](auto M) {
      constexpr int m = decltype(M)::value, rb = m / 8, db = m % 8;
      mfma_acc<AO(rb, db)>(vf[db], Pf(rb));
      if constexpr (rb + 1 < NRB) {
        constexpr int k0 = db < 4 ? 2 * db : db < 6 ? 4 + db : 10 + 2 * (db - 6), k1 = db < 4 ? 2 * db + 2 : db < 6 ? 5 + db : 12 + 2 * (db - 6);
#pragma unroll
        for (int k = k0; k < k1; ++k) sm_item(rb + 1, k);
        if constexpr (db == 7) asm volatile("s_nop 1" ::: "memory");
      }
      SB();
    });
  };
#pragma unroll
  for (int rb = 0; rb < NRB; ++rb) { S[rb][0] = f32x4{-1e30f, -1e30f, -1e30f, -1e30f}; S[rb][1] = S[rb][0]; Pw[rb][0] = Pw[rb][1] = Pw[rb][2] = Pw[rb][3] = 0u; }
#pragma unroll
  for (int db = 0; db < 8; ++db) { vf[db] = s16x8{0, 0, 0, 0, 0, 0, 0, 0}; kf[db >> 2][db & 3] = vf[db]; }
  auto wait_vf = [&]() { asm volatile("s_waitcnt lgkmcnt(0)" : "+v"(vf[0]), "+v"(vf[1]), "+v"(vf[2]), "+v"(vf[3]), "+v"(vf[4]), "+v"(vf[5]), "+v"(vf[6]), "+v"(vf[7]) :: "memory"); };
  auto aiter = [&](int j, auto PHC) {
    constexpr int PH = decltype(PHC)::value;
    constexpr int KS = 2 * ((PH + 1) % 4), VS = KS + 1, KD = 2 * PH, VD = KD + 1;
    TS(tw0);
    if constexpr (SMALL) { if (j < 30) WAITV(16); else WAITV(0); }
    BARRIER();
    TS(tw1); twsum += tw1 - tw0;
    do_kread(IC<KS>{});
    if constexpr (SMALL) {
      if (j + 3 < 32) tileK(IC<KD>{}, kg + (size_t)(j + 3) * 8192);
      SB();
      do_smpv(IC<NRB>{});
      TS2(tw0); sA += tw0 - tw1;
      do_qk(IC<VS>{}, IC<1>{});
      TS2(tw1); sB += tw1 - tw0;
      do_check(j == 0);
      TS2(tw0); sC += tw0 - tw1;
      if (j + 3 < 32) tileV(IC<VD>{}, vg + (size_t)(j + 3) * 8192);
      wait_vf();
    } else {
      do_qk(IC<VS>{}, IC<1>{});
      TS2(tw0); sA += tw0 - tw1;
      do_check(j == 0);
      TS2(tw1); sB += tw1 - tw0;
      do_smpv(IC<NRB>{});
      TS2(tw0); sC += tw0 - tw1;
    }
  };
  for (int j = 0; j < 32; j += 4) { aiter(j, IC<0>{}); aiter(j + 1, IC<1>{}); aiter(j + 2, IC<2>{}); aiter(j + 3, IC<3>{}); }
  unsigned rp[2][8];
  {
    const unsigned ro0 = (4 * g) * 1024 + li * 2, ro1 = ro0 + 16384;
    sfor<2>([&](auto F) { sfor<8>([&](auto J) {
      constexpr int f = decltype(F)::value, j = decltype(J)::value;
      rp[f][j] = gload_u16<(j & 3) * 1024 + f * 32>(j < 4 ? ro0 : ro1, wsrc);
    }); });
  }
  if constexpr (SMALL) do_smpv(IC<NRB>{});

  TS(ts3);
  asm volatile("s_nop 7\n\ts_nop 7" ::: "memory");
  sfor<NRB>([&](auto R) {
    constexpr int rb = decltype(R)::value;
    float l = lsum[rb]; l += __shfl_xor(l, 16); l += __shfl_xor(l, 32);
    const float il = __builtin_amdgcn_rcpf(l);
    sfor<4>([&](auto Tt) {
      constexpr int t = decltype(Tt)::value;
      float v[8];
      sfor<8>([&](auto E) { v[decltype(E)::value] = acc_rd<AO(rb, 2 * t) + decltype(E)::value>() * il; });
      u32x4 o = {cvtpk(v[0], v[1]), cvtpk(v[2], v[3]), cvtpk(v[4], v[5]), cvtpk(v[6], v[7])};
      *(u32x4*)(ob + (size_t)rb * (NH * 4 * 1024) + t * 1024) = o;
    });
  });
  asm volatile("s_waitcnt vmcnt(%c16)" : "+v"(rp[0][0]), "+v"(rp[0][1]), "+v"(rp[0][2]), "+v"(rp[0][3]), "+v"(rp[0][4]), "+v"(rp[0][5]), "+v"(rp[0][6]), "+v"(rp[0][7]),
               "+v"(rp[1][0]), "+v"(rp[1][1]), "+v"(rp[1][2]), "+v"(rp[1][3]), "+v"(rp[1][4]), "+v"(rp[1][5]), "+v"(rp[1][6]), "+v"(rp[1][7]) : "i"(NRB * 4) : "memory");
#pragma unroll
  for (int f = 0; f < 2; ++f) {
    u32x4 v = {rp[f][0] | (rp[f][1] << 16), rp[f][2] | (rp[f][3] << 16), rp[f][4] | (rp[f][5] << 16), rp[f][6] | (rp[f][7] << 16)};
    *(u32x4*)(wdst + f * 1024 + lane * 16) = v;
  }
}

__global__ __launch_bounds__(512, 2)
void attn_kernel(const __hip_bfloat16* __restrict__ xA, const __hip_bfloat16* __restrict__ kA, const __hip_bfloat16* __restrict__ vA, const __hip_bfloat16* __restrict__ wqA,
                 const __hip_bfloat16* __restrict__ xB, const __hip_bfloat16* __restrict__ kB, const __hip_bfloat16* __restrict__ vB, const __hip_bfloat16* __restrict__ wqB,
                 const __hip_bfloat16* __restrict__ woA, const __hip_bfloat16* __restrict__ woB, __hip_bfloat16* __restrict__ ows) {
  __shared__ __attribute__((aligned(16))) char smem[20 * TILE];
  const unsigned lds0 = (unsigned)(size_t)smem;
  const int tid = threadIdx.x, lane = tid & 63;
  const int w = __builtin_amdgcn_readfirstlane(tid >> 6);
  const int e = w & 1, r = (w >> 1) & 1;
  const int idx = blockIdx.x, xcd = idx & 7, j0 = idx >> 3, blk = j0 >> 5, jj = j0 & 31;
  const int b = (xcd >> 2) * 8 + (jj >> 2), h = ((xcd & 3) * 4 + (jj & 3)) * 2 + e;
  const char* xg = (const char*)(blk ? xB : xA) + (size_t)b * SQ * DM * 2;
  const char* wqg = (const char*)(blk ? wqB : wqA) + (size_t)h * DH * 2;
  const char* kg = (const char*)(blk ? kB : kA) + ((size_t)b * NH + h) * SK * DH * 2 + r * 4096;
  const char* vg = (const char*)(blk ? vB : vA) + ((size_t)b * NH + h) * SK * DH * 2 + r * 4096;
  const unsigned ldsh = lds0 + e * (8 * TILE);
  const int rbg = w < 4 ? 3 * r : 6 + 2 * r;
  char* ob = (char*)ows + ((((size_t)(blk * NB + b) * 10 + rbg) * NH + h) * 4) * 1024 + lane * 16;
  if (idx < 256 && tid == 0) ((unsigned*)((char*)ows + WS_FLAG))[idx] = 0u;
  const int rpp = idx & 255, rnt = rpp & 7, rks = (rpp >> 3) * 4 + (w >> 1);
  const unsigned short* wsrc = (const unsigned short*)((idx >> 8) ? woB : woA) + (size_t)rks * 32 * DM + rnt * 64 + (w & 1) * 32;
  char* wdst = (char*)ows + WS_WO + ((size_t)((idx >> 8) * 8 + rnt) * 128 + rks) * 4096 + (w & 1) * 2048;
  if (w < 4) attn_body<3>(wsrc, wdst, xg, wqg, kg, vg, ob, lds0, ldsh, r, w & 3, 16 * rbg, lane); else attn_body<2>(wsrc, wdst, xg, wqg, kg, vg, ob, lds0, ldsh, r, w & 3, 16 * rbg, lane);
}

constexpr int K2_STG = 36864;
DI void dma_rt(unsigned ldsaddr, unsigned v0, const void* gbase) {
  asm volatile("s_mov_b32 m0, %0\n\ts_nop 0\n\tglobal_load_lds_dwordx4 %1, %2" :: "s"(ldsaddr), "v"(v0), "s"(gbase) : "memory");
}
__global__ __launch_bounds__(512, 2)
void oproj_kernel(char* __restrict__ ws, __hip_bfloat16* __restrict__ out) {
  __shared__ __attribute__((aligned(16))) char smem[4 * K2_STG];
  const unsigned lds0 = (unsigned)(size_t)smem;
  const int tid = threadIdx.x, lane = tid & 63;
  const int w = __builtin_amdgcn_readfirstlane(tid >> 6);
  const int kq = w >> 2, rh = (w >> 1) & 1, nh = w & 1;
  const int li = lane & 15, g = lane >> 4;
  const int idx = blockIdx.x, xcd = idx & 7, r = idx >> 3;
  const int bb = xcd * 4 + (r >> 3), nt2 = (r >> 1) & 3, kh = r & 1;
  const int blk = bb >> 4;
  const unsigned voff = lane * 16;
  const char* src[5]; unsigned dst[5]; unsigned adv[5];
#pragma unroll
  for (int i = 0; i < 5; ++i) {
    const int pp = w + 8 * i < 36 ? w + 8 * i : w;
    if (pp < 20) { const int kk = pp / 10, rb = pp % 10;
      src[i] = ws + ((size_t)bb * 10 + rb) * 131072 + (size_t)(kh * 64 + kk) * 1024; dst[i] = lds0 + kk * 18432 + rb * 1024; adv[i] = 2048; }
    else { const int q = pp - 20, kk = q >> 3, cb = q & 7;
      src[i] = ws + WS_WO + ((size_t)(blk * 8 + nt2 * 2 + (cb >> 2)) * 128 + kh * 64 + kk) * 4096 + (cb & 3) * 1024; dst[i] = lds0 + kk * 18432 + 10240 + cb * 1024; adv[i] = 8192; }
  }
  auto issue = [&](int st, int D) {
#pragma unroll
    for (int i = 0; i < 4; ++i) dma_rt(dst[i] + st * K2_STG, voff, src[i] + (size_t)D * adv[i]);
    if (w < 4) dma_rt(dst[4] + st * K2_STG, voff, src[4] + (size_t)D * adv[4]);
  };
  const unsigned oadr = lds0 + kq * 18432 + rh * 5120 + lane * 16, wadr = lds0 + kq * 18432 + 10240 + nh * 4096 + lane * 16;
  issue(0, 0); issue(1, 1); issue(2, 2);
  asm volatile("" ::: ACLOB);
  sfor<20>([&](auto I) { acc_zero4<4 * decltype(I)::value>(); });
  s16x8 of[2][5], wf[2][4];
  auto rd = [&](auto BC, auto STC, auto IT) { constexpr int bf = decltype(BC)::value, st = decltype(STC)::value, it = decltype(IT)::value;
    if constexpr (it < 4) wf[bf][it] = lds_b128(wadr + st * K2_STG + it * 1024); else of[bf][it - 4] = lds_b128(oadr + st * K2_STG + (it - 4) * 1024); };
  if (w < 4) WAITV(10); else WAITV(8);
  BARRIER();
  sfor<9>([&](auto I) { rd(IC<0>{}, IC<0>{}, I); });
  auto step = [&](auto PHC, int D) {
    constexpr int PH = decltype(PHC)::value;
    if (D < 29) { if (w < 4) asm volatile("s_waitcnt vmcnt(5) lgkmcnt(0)" ::: "memory"); else asm volatile("s_waitcnt vmcnt(4) lgkmcnt(0)" ::: "memory"); }
    else asm volatile("s_waitcnt vmcnt(0) lgkmcnt(0)" ::: "memory");
    BARRIER();
    SB();
    if (D + 3 < 32) issue((PH + 3) % 4, D + 3);
    SB();
    sfor<20>([&](auto M) { constexpr int m = decltype(M)::value, nb = m / 5, rb = m % 5;
      mfma_acc<(rb * 4 + nb) * 4>(wf[PH & 1][nb], of[PH & 1][rb]);
      if constexpr (m < 9) rd(IC<(PH + 1) & 1>{}, IC<(PH + 1) % 4>{}, IC<m>{});
      SB();
    });
  };
  for (int D = 0; D < 32; D += 4) { step(IC<0>{}, D); step(IC<1>{}, D + 1); step(IC<2>{}, D + 2); step(IC<3>{}, D + 3); }
  asm volatile("s_waitcnt lgkmcnt(0)\n\ts_nop 7\n\ts_nop 7" ::: "memory");
  __syncthreads();
  f32x4 v[20];
  sfor<20>([&](auto F) { constexpr int f = decltype(F)::value; v[f] = f32x4{acc_rd<f * 4>(), acc_rd<f * 4 + 1>(), acc_rd<f * 4 + 2>(), acc_rd<f * 4 + 3>()}; });
  if (kq == 1) {
#pragma unroll
    for (int f = 0; f < 20; ++f) *(lds_f32x4_ptr)(lds0 + ((w & 3) * 20 + f) * 1024 + lane * 16) = v[f];
  }
  __syncthreads();
  char* xbuf = ws + WS_XCH;
  unsigned* flags = (unsigned*)(ws + WS_FLAG);
  if (kq == 0) {
#pragma unroll
    for (int f = 0; f < 20; ++f) v[f] += *(lds_f32x4_ptr)(lds0 + (w * 20 + f) * 1024 + lane * 16);
    if (nh != kh) {
      char* dstp = xbuf + ((size_t)idx * 2 + rh) * 20480 + lane * 16;
#pragma unroll
      for (int f = 0; f < 20; ++f) asm volatile("global_store_dwordx4 %0, %1, off sc1\n\ts_nop 1" :: "v"(dstp + f * 1024), "v"(v[f]) : "memory");
      asm volatile("s_waitcnt vmcnt(0)" ::: "memory");
    }
  }
  __syncthreads();
  if (tid == 0) __hip_atomic_store(flags + idx, 1u, __ATOMIC_RELAXED, __HIP_MEMORY_SCOPE_AGENT);
  if (kq == 0 && nh == kh) {
    const int pidx = idx ^ 8;
    for (int spin = 0; spin < (1 << 22); ++spin) {
      if (__hip_atomic_load(flags + pidx, __ATOMIC_RELAXED, __HIP_MEMORY_SCOPE_AGENT) != 0u) break;
      __builtin_amdgcn_s_sleep(2);
    }
    __builtin_amdgcn_fence(__ATOMIC_ACQUIRE, "agent");
    const char* srcp = xbuf + ((size_t)pidx * 2 + rh) * 20480 + lane * 16;
    char* outb = (char*)out + ((size_t)(bb & 15) * SQ + rh * 80) * 2048 + blk * 1024 + nt2 * 256 + nh * 128 + g * 8;
#pragma unroll
    for (int c = 0; c < 4; ++c) {
      f32x4 pv[5];
#pragma unroll
      for (int f = 0; f < 5; ++f) asm volatile("global_load_dwordx4 %0, %1, off sc1" : "=v"(pv[f]) : "v"(srcp + (c * 5 + f) * 1024) : "memory");
      asm volatile("s_waitcnt vmcnt(0)" : "+v"(pv[0]), "+v"(pv[1]), "+v"(pv[2]), "+v"(pv[3]), "+v"(pv[4]) :: "memory");
#pragma unroll
      for (int f5 = 0; f5 < 5; ++f5) {
        const int f = c * 5 + f5;
        const f32x4 sacc = v[f] + pv[f5];
        uint2 o2; o2.x = cvtpk(sacc[0], sacc[1]); o2.y = cvtpk(sacc[2], sacc[3]);
        *(uint2*)(outb + (size_t)(16 * (f >> 2) + li) * 2048 + (f & 3) * 32) = o2;
      }
    }
  }
}

extern "C" void kernel_launch(void* const* d_in, const int* in_sizes, int n_in,
                              void* d_out, int out_size, void* d_ws, size_t ws_size,
                              hipStream_t stream) {
  const __hip_bfloat16* xA = (const __hip_bfloat16*)d_in[0];
  const __hip_bfloat16* kA = (const __hip_bfloat16*)d_in[1];
  const __hip_bfloat16* vA = (const __hip_bfloat16*)d_in[2];
  const __hip_bfloat16* wqA = (const __hip_bfloat16*)d_in[3];
  const __hip_bfloat16* woA = (const __hip_bfloat16*)d_in[4];
  const __hip_bfloat16* xB = (const __hip_bfloat16*)d_in[5];
  const __hip_bfloat16* kB = (const __hip_bfloat16*)d_in[6];
  const __hip_bfloat16* vB = (const __hip_bfloat16*)d_in[7];
  const __hip_bfloat16* wqB = (const __hip_bfloat16*)d_in[8];
  const __hip_bfloat16* woB = (const __hip_bfloat16*)d_in[9];
  __hip_bfloat16* ows = (__hip_bfloat16*)d_ws;
  attn_kernel<<<512, 512, 0, stream>>>(xA, kA, vA, wqA, xB, kB, vB, wqB, woA, woB, ows);
  oproj_kernel<<<256, 512, 0, stream>>>((char*)d_ws, (__hip_bfloat16*)d_out);
}
```

```cpp
#include <hip/hip_runtime.h>
#include <hip/hip_bf16.h>
#include <utility>
#include <type_traits>

typedef __attribute__((ext_vector_type(8))) short s16x8;
typedef __attribute__((ext_vector_type(4))) short s16x4;
typedef __attribute__((ext_vector_type(4))) float f32x4;
typedef __attribute__((ext_vector_type(2))) float f32x2;
typedef __attribute__((ext_vector_type(2))) __bf16 bf16x2;
typedef __attribute__((ext_vector_type(4))) unsigned u32x4;
typedef __attribute__((address_space(3))) s16x4* lds_s16x4_ptr;
typedef __attribute__((address_space(3))) s16x8* lds_s16x8_ptr;
typedef __attribute__((address_space(3))) f32x4* lds_f32x4_ptr;

#define DI __device__ __forceinline__

constexpr int NB = 16, SQ = 160, DM = 512, NH = 32, SK = 1024, DH = 128;
constexpr int NS = 5;
constexpr int SLOT = 16384;
constexpr float QSCALE = 0.088388347648318440f * 1.4426950408889634f;
constexpr size_t WS_WO = (size_t)48 << 20;
constexpr float THR = 8.0f;

DI unsigned cvtpk(float lo, float hi) { f32x2 v = {lo, hi}; bf16x2 b = __builtin_convertvector(v, bf16x2); return __builtin_bit_cast(unsigned, b); }
DI s16x8 pack8(f32x4 a, f32x4 b) { u32x4 w = {cvtpk(a[0], a[1]), cvtpk(a[2], a[3]), cvtpk(b[0], b[1]), cvtpk(b[2], b[3])}; return __builtin_bit_cast(s16x8, w); }
DI float max3f(float a, float b, float c) { float r; asm("v_max3_f32 %0, %1, %2, %3" : "=v"(r) : "v"(a), "v"(b), "v"(c)); return r; }
DI float max2f(float a, float b) { float r; asm("v_max_f32_e32 %0, %1, %2" : "=v"(r) : "v"(a), "v"(b)); return r; }
DI f32x4 mfma16(s16x8 a, s16x8 b, f32x4 c) { return __builtin_amdgcn_mfma_f32_16x16x32_bf16(a, b, c, 0, 0, 0); }
DI s16x8 lds_b128(unsigned addr) { return *(lds_s16x8_ptr)(addr); }
DI s16x4 lds_tr(unsigned addr) { return __builtin_amdgcn_ds_read_tr16_b64_v4i16((lds_s16x4_ptr)(addr)); }
DI s16x8 cat8(s16x4 a, s16x4 b) { return __builtin_shufflevector(a, b, 0, 1, 2, 3, 4, 5, 6, 7); }

DI void dma4(unsigned ldsaddr, unsigned v0, unsigned v1, unsigned v2, unsigned v3, const void* gbase) {
  asm volatile("s_mov_b32 m0, %0\n\ts_nop 0\n\t"
               "global_load_lds_dwordx4 %1, %5 nt\n\t"
               "global_load_lds_dwordx4 %2, %5 offset:1024 nt\n\t"
               "global_load_lds_dwordx4 %3, %5 offset:2048 nt\n\t"
               "global_load_lds_dwordx4 %4, %5 offset:3072 nt"
               :: "s"(ldsaddr), "v"(v0), "v"(v1), "v"(v2), "v"(v3), "s"(gbase) : "memory");
}
DI void dma1(unsigned ldsaddr, unsigned v0, const void* gbase) {
  asm volatile("s_mov_b32 m0, %0\n\ts_nop 0\n\t"
               "global_load_lds_dwordx4 %1, %2"
               :: "s"(ldsaddr), "v"(v0), "s"(gbase) : "memory");
}
template <int OFF> DI s16x8 gload16(unsigned voff, const void* gbase) {
  s16x8 r; asm volatile("global_load_dwordx4 %0, %1, %2 offset:%3" : "=v"(r) : "v"(voff), "s"(gbase), "i"(OFF) : "memory"); return r;
}
template <int OFF> DI unsigned gload_u16(unsigned voff, const void* gbase) {
  unsigned r; asm volatile("global_load_ushort %0, %1, %2 offset:%3" : "=v"(r) : "v"(voff), "s"(gbase), "i"(OFF) : "memory"); return r;
}
#define WAITV(n) asm volatile("s_waitcnt vmcnt(" #n ")" ::: "memory")
#define BARRIER() asm volatile("s_barrier" ::: "memory")
#define PIN5(a) asm volatile("" : "+v"(a[0]), "+v"(a[1]), "+v"(a[2]), "+v"(a[3]), "+v"(a[4]))

#define ACLOB "a0","a1","a2","a3","a4","a5","a6","a7","a8","a9","a10","a11","a12","a13","a14","a15","a16","a17","a18","a19","a20","a21","a22","a23","a24","a25","a26","a27","a28","a29","a30","a31","a32","a33","a34","a35","a36","a37","a38","a39","a40","a41","a42","a43","a44","a45","a46","a47","a48","a49","a50","a51","a52","a53","a54","a55","a56","a57","a58","a59","a60","a61","a62","a63","a64","a65","a66","a67","a68","a69","a70","a71","a72","a73","a74","a75","a76","a77","a78","a79","a80","a81","a82","a83","a84","a85","a86","a87","a88","a89","a90","a91","a92","a93","a94","a95","a96","a97","a98","a99","a100","a101","a102","a103","a104","a105","a106","a107","a108","a109","a110","a111","a112","a113","a114","a115","a116","a117","a118","a119","a120","a121","a122","a123","a124","a125","a126","a127"
#define SB() __builtin_amdgcn_sched_barrier(0)
template <int... I, class F> DI void sfor_impl(std::integer_sequence<int, I...>, F&& f) { (f(std::integral_constant<int, I>{}), ...); }
template <int N, class F> DI void sfor(F&& f) { sfor_impl(std::make_integer_sequence<int, N>{}, f); }
template <int N> using IC = std::integral_constant<int, N>;
constexpr int AO(int rb, int db) { return (rb * 8 + db) * 4; }
constexpr int AQ(int f) { return 96 + f * 4; }
template <int A> DI void mfma_acc(s16x8 a, s16x8 b) {
  asm volatile("v_mfma_f32_16x16x32_bf16 a[%c2:%c3], %0, %1, a[%c2:%c3]" :: "v"(a), "v"(b), "i"(A), "i"(A + 3));
}
template <int A> DI void mfma_qk0(f32x4& S, s16x8 kf, const f32x4& nm) {
  asm volatile("v_mfma_f32_16x16x32_bf16 %0, %1, a[%c3:%c4], %2" : "=&v"(S) : "v"(kf), "v"(nm), "i"(A), "i"(A + 3));
}
template <int A> DI void mfma_qk(f32x4& S, s16x8 kf) {
  asm volatile("v_mfma_f32_16x16x32_bf16 %0, %1, a[%c2:%c3], %0" : "+v"(S) : "v"(kf), "i"(A), "i"(A + 3));
}
DI void mfma_v0(f32x4& D, s16x8 a, s16x8 b, const f32x4& c) { asm volatile("v_mfma_f32_16x16x32_bf16 %0, %1, %2, %3" : "=&v"(D) : "v"(a), "v"(b), "v"(c)); }
DI void mfma_v(f32x4& D, s16x8 a, s16x8 b) { asm volatile("v_mfma_f32_16x16x32_bf16 %0, %1, %2, %0" : "+v"(D) : "v"(a), "v"(b)); }
template <int A> DI float acc_rd() { float r; asm volatile("v_accvgpr_read_b32 %0, a[%c1]" : "=v"(r) : "i"(A)); return r; }
template <int A> DI void acc_wr(unsigned v) { asm volatile("v_accvgpr_write_b32 a[%c1], %0" :: "v"(v), "i"(A)); }
template <int A> DI void acc_zero4() { asm volatile("v_accvgpr_write_b32 a[%c0], 0\n\tv_accvgpr_write_b32 a[%c1], 0\n\tv_accvgpr_write_b32 a[%c2], 0\n\tv_accvgpr_write_b32 a[%c3], 0" :: "i"(A), "i"(A + 1), "i"(A + 2), "i"(A + 3)); }
template <int A> DI void acc_scale(float f) { float t; asm volatile("v_accvgpr_read_b32 %0, a[%c2]\n\tv_mul_f32 %0, %0, %1\n\tv_accvgpr_write_b32 a[%c2], %0" : "=&v"(t) : "v"(f), "i"(A)); }
DI void add4(float& l, const f32x4& v) { asm volatile("v_add_f32_e32 %0, %0, %1\n\tv_add_f32_e32 %0, %0, %2\n\tv_add_f32_e32 %0, %0, %3\n\tv_add_f32_e32 %0, %0, %4" : "+v"(l) : "v"(v[0]), "v"(v[1]), "v"(v[2]), "v"(v[3])); }
DI float fadd_s(float a, float b) { float r; asm("v_add_f32_e32 %0, %1, %2" : "=v"(r) : "v"(a), "v"(b)); return r; }
template <int LOFF> DI void dma4_nt(unsigned ldsw, unsigned v0, unsigned v1, unsigned v2, unsigned v3, const void* gbase) {
  asm volatile("s_add_u32 m0, %0, %6\n\ts_nop 0\n\t"
               "global_load_lds_dwordx4 %1, %5 nt\n\t"
               "global_load_lds_dwordx4 %2, %5 offset:1024 nt\n\t"
               "global_load_lds_dwordx4 %3, %5 offset:2048 nt\n\t"
               "global_load_lds_dwordx4 %4, %5 offset:3072 nt"
               :: "s"(ldsw), "v"(v0), "v"(v1), "v"(v2), "v"(v3), "s"(gbase), "i"(LOFF) : "memory", "scc");
}
template <int LOFF> DI void dma_m0(unsigned ldsw, unsigned v0, const void* gbase) {
  asm volatile("s_add_u32 m0, %0, %3\n\ts_nop 0\n\tglobal_load_lds_dwordx4 %1, %2" :: "s"(ldsw), "v"(v0), "s"(gbase), "i"(LOFF) : "memory", "scc");
}
#define TS(v) do { } while (0)
#define TS2(v) do { } while (0)
constexpr int TILE = 8192;
constexpr int XBUF(int c) { return (c % 3) == 0 ? 40 * 1024 : (c % 3) == 1 ? 140 * 1024 : 104 * 1024; }

template <int NRB>
DI void attn_body(const unsigned short* wsrc, const unsigned short* wsrc1, char* wdst, const char* xg, const char* xg1, const char* wqg, const char* wqg1, const char* kg, const char* kg1, const char* vg, const char* vg1, char* ob, const unsigned lds0, const unsigned ldsh, const int r, const int bw, const int row0, const int lane
                  ) {
  constexpr bool SMALL = NRB == 2;
  const unsigned ldsw = ldsh + r * 4096;
  const int hw = SMALL ? 2 + r : r;
  const unsigned ldsq = ldsh + hw * 2048;
  const char* wqw = wqg + (size_t)hw * 8 * 8192;
  unsigned voffK[4], voffV[2], voffW, wofs[4], voffX[2], xofs[2];
  auto addr_dma_q = [&]() {
    int l = lane; asm volatile("" : "+v"(l)); const int li = l & 15, g = l >> 4;
    voffW = g * 8192 + ((li ^ ((g & 1) | ((g >> 1) << 2) | (r << 3))) * 16);
#pragma unroll
    for (int n = 0; n < 2; ++n) voffX[n] = (l >> 3) * 1024 + (((l & 7) ^ (4 * ((n + bw) & 1) + g)) * 16);
  };
  auto addr_q = [&]() {
    int l = lane; asm volatile("" : "+v"(l)); const int li = l & 15, g = l >> 4, q4 = (l >> 2) & 3, p4 = l & 3;
#pragma unroll
    for (int n = 0; n < 4; ++n) voffK[n] = g * 256 + (((li ^ g) ^ (4 * n)) * 16);
#pragma unroll
    for (int n = 0; n < 2; ++n) voffV[n] = g * 256 + ((li ^ (g << 1) ^ (n << 3)) * 16);
#pragma unroll
    for (int t = 0; t < 4; ++t) wofs[t] = ldsh + (8 * g + q4) * 256 + (((4 * t + p4) ^ ((q4 & 1) | ((q4 >> 1) << 2) | ((g & 1) << 3))) * 16) + (p4 & 1) * 8;
#pragma unroll
    for (int kk = 0; kk < 2; ++kk) xofs[kk] = lds0 + (row0 + li) * 128 + (((4 * kk + g) ^ (li >> 1)) * 16);
  };

  unsigned long long tqa = 0, tqb = 0, sA = 0, sB = 0, sC = 0; (void)tqa; (void)tqb; (void)sA; (void)sB; (void)sC;
  unsigned long long ts0 = 0, ts1 = 0, ts3 = 0, ts4 = 0, tw0 = 0, tw1 = 0, twsum = 0, tqw = 0;
  (void)ts0; (void)ts1; (void)ts3; (void)ts4; (void)tw0; (void)tw1; (void)twsum; (void)tqw;
  TS(ts0);

  auto pieceW = [&](auto N, auto I) { constexpr int n = decltype(N)::value, i = decltype(I)::value;
    dma_m0<(n % 5) * TILE + i * 1024>(ldsq, voffW, wqw + (size_t)(32 * n) * 8192 + (size_t)i * 32768); };
  auto tileW = [&](auto N) { pieceW(N, IC<0>{}); pieceW(N, IC<1>{}); };
  auto tileK = [&](auto SL, const char* gb) { dma4_nt<decltype(SL)::value * TILE>(ldsw, voffK[0], voffK[1], voffK[2], voffK[3], gb); };
  auto tileV = [&](auto SL, const char* gb) { dma4_nt<decltype(SL)::value * TILE>(ldsw, voffV[0], voffV[1], voffV[0], voffV[1], gb); };

  const unsigned ldsx = lds0 + bw * 5120;
  const char* xgw = xg + (size_t)bw * 40 * 1024;
  auto pieceX = [&](auto C, auto PC) { constexpr int c = decltype(C)::value, pc = decltype(PC)::value;
    dma_m0<XBUF(c) + pc * 1024>(ldsx, voffX[pc & 1], xgw + (size_t)pc * 8192 + c * 128); };
  auto chunkX = [&](auto C) { sfor<5>([&](auto PC) { pieceX(C, PC); }); };
  auto prologue = [&]() {
    addr_dma_q();
    if constexpr (!SMALL) chunkX(IC<0>{});
    tileW(IC<0>{}); tileW(IC<1>{});
    if constexpr (!SMALL) chunkX(IC<1>{});
    tileW(IC<2>{});
    if constexpr (!SMALL) chunkX(IC<2>{});
    tileW(IC<3>{}); tileW(IC<4>{});
  };
  prologue();
  for (int task = 0; task < 2; ++task) {
  addr_q();
  asm volatile("" ::: ACLOB);
  sfor<NRB * 8>([&](auto I) { acc_zero4<4 * decltype(I)::value>(); });
  s16x8 wf[2][8], xf[2][NRB];
  auto wread = [&](auto N, auto I) { constexpr int n = decltype(N)::value, i = decltype(I)::value;
    const unsigned a = (wofs[i >> 1] ^ ((i & 1) * 8)) + (n % 5) * TILE; wf[n & 1][i] = cat8(lds_tr(a), lds_tr(a + 1024)); };
  auto xread = [&](auto N, auto R) { constexpr int n = decltype(N)::value, rb = decltype(R)::value;
    xf[n & 1][rb] = lds_b128(xofs[n & 1] + XBUF(n >> 1) + rb * 2048); };
  if constexpr (SMALL) WAITV(8); else WAITV(18);
  BARRIER();
  TS(tqa);
  sfor<NRB>([&](auto R) { xread(IC<0>{}, R); });
  sfor<8>([&](auto I) { wread(IC<0>{}, I); });
  auto qstep = [&](auto N, auto WSC) {
    constexpr int n = decltype(N)::value;
    constexpr int WN = SMALL ? decltype(WSC)::value : (n == 0 ? 16 : n <= 3 ? 11 : n <= 11 ? ((n & 1) ? 11 : 16) : n == 12 ? 9 : n == 13 ? 2 : n == 14 ? 0 : 63);
    TS(tw0);
    asm volatile("s_waitcnt vmcnt(%0) lgkmcnt(0)" :: "i"(WN) : "memory");
    BARRIER();
    TS(tw1); tqw += tw1 - tw0;
    SB();
    constexpr bool hasW = n + 5 < 16, hasX = !SMALL && (n & 1) == 1 && (n + 5) / 2 < 8, hasKV = SMALL && n >= 12;
    sfor<8 * NRB>([&](auto M) { constexpr int m = decltype(M)::value, d = m / NRB, rb = m % NRB;
      mfma_acc<AO(rb, d)>(wf[n & 1][d], xf[n & 1][rb]);
      if constexpr (n < 15) {
        if constexpr (m < NRB) xread(IC<n + 1>{}, IC<m>{});
        else if constexpr (m < NRB + 8) wread(IC<n + 1>{}, IC<m - NRB>{});
      }
      if constexpr (hasW && (m == 1 || m == 3)) pieceW(IC<hasW ? n + 5 : 0>{}, IC<m / 2>{});
      if constexpr (hasX && m >= 5 && m <= 13 && (m & 1)) pieceX(IC<hasX ? (n + 5) / 2 : 0>{}, IC<(m - 5) / 2>{});
      if constexpr (hasKV && m == 1) {
        if constexpr (n == 12) tileK(IC<2>{}, kg);
        if constexpr (n == 13) tileV(IC<3>{}, vg);
        if constexpr (n == 14) { tileK(IC<4>{}, kg + 8192); tileV(IC<5>{}, vg + 8192); }
        if constexpr (n == 15) { tileK(IC<6>{}, kg + 16384); tileV(IC<7>{}, vg + 16384); }
      }
      SB();
    });
  };
  qstep(IC<0>{}, IC<6>{}); qstep(IC<1>{}, IC<6>{}); qstep(IC<2>{}, IC<6>{}); qstep(IC<3>{}, IC<6>{});
  qstep(IC<4>{}, IC<6>{}); qstep(IC<5>{}, IC<6>{}); qstep(IC<6>{}, IC<6>{}); qstep(IC<7>{}, IC<6>{});
  qstep(IC<8>{}, IC<6>{}); qstep(IC<9>{}, IC<6>{}); qstep(IC<10>{}, IC<6>{}); qstep(IC<11>{}, IC<6>{});
  qstep(IC<12>{}, IC<4>{}); qstep(IC<13>{}, IC<6>{}); qstep(IC<14>{}, IC<8>{}); qstep(IC<15>{}, IC<63>{});
  TS(tqb);
  asm volatile("s_nop 7\n\ts_nop 7" ::: "memory");
  s16x8 Qv[4];
  sfor<NRB>([&](auto R) { sfor<4>([&](auto Tt) {
    constexpr int rb = decltype(R)::value, t = decltype(Tt)::value, f = rb * 4 + t;
    float v[8];
    sfor<8>([&](auto E) { v[decltype(E)::value] = acc_rd<AO(rb, 2 * t) + decltype(E)::value>() * QSCALE; });
    const unsigned c0 = cvtpk(v[0], v[1]), c1 = cvtpk(v[2], v[3]), c2 = cvtpk(v[4], v[5]), c3 = cvtpk(v[6], v[7]);
    const bool odd = (lane & 16) != 0;
    const unsigned q0 = odd ? c2 : c0, q1 = odd ? c3 : c1, q2 = odd ? c0 : c2, q3 = odd ? c1 : c3;
    if constexpr (f < 8) { acc_wr<AQ(f)>(q0); acc_wr<AQ(f) + 1>(q1); acc_wr<AQ(f) + 2>(q2); acc_wr<AQ(f) + 3>(q3); }
    else { u32x4 q = {q0, q1, q2, q3}; Qv[f - 8] = __builtin_bit_cast(s16x8, q); }
  }); });
  sfor<NRB * 8>([&](auto I) { acc_zero4<4 * decltype(I)::value>(); });

  unsigned kofs[4], vofs[8];
  {
    int l2 = lane; asm volatile("" : "+v"(l2));
    const int li2 = l2 & 15, g2 = l2 >> 4, q2 = (l2 >> 2) & 3, p2 = l2 & 3;
#pragma unroll
    for (int ks = 0; ks < 4; ++ks) kofs[ks] = ldsh + li2 * 256 + (((4 * ks + g2) ^ li2) * 16);
    const unsigned r7 = 4 * (g2 & 1) + q2;
#pragma unroll
    for (int db = 0; db < 8; ++db) vofs[db] = ldsh + (4 * g2 + q2) * 256 + (r7 ^ db) * 32 + (p2 >> 1) * 16 + (p2 & 1) * 8;
  }
  f32x4 NM[NRB], S[NRB][2];
  float lsum[NRB];
  unsigned Pw[NRB][4];
#pragma unroll
  for (int rb = 0; rb < NRB; ++rb) { lsum[rb] = 0.f; NM[rb] = f32x4{0.f, 0.f, 0.f, 0.f}; }
  auto rowmax = [&](const f32x4 (&Sr)[2]) {
    float t = max3f(Sr[0][0], Sr[0][1], Sr[0][2]);
    t = max3f(t, Sr[0][3], Sr[1][0]); t = max3f(t, Sr[1][1], Sr[1][2]); t = max2f(t, Sr[1][3]);
    return t;
  };
  auto xlane_max = [&](float t) { t = fmaxf(t, __shfl_xor(t, 16)); t = fmaxf(t, __shfl_xor(t, 32)); return t; };
  auto sm_item = [&](int rb, int k) {
    if (k < 8) { float t = __builtin_amdgcn_exp2f(S[rb][k >> 2][k & 3]); asm volatile("" : "+v"(t)); S[rb][k >> 2][k & 3] = t; }
    else if (k < 10) add4(lsum[rb], S[rb][k - 8]);
    else { const int c = k - 10; unsigned t = cvtpk(S[rb][c >> 1][(c & 1) * 2], S[rb][c >> 1][(c & 1) * 2 + 1]); asm volatile("" : "+v"(t)); Pw[rb][c] = t; }
  };
  auto Pf = [&](int rb) { u32x4 v = {Pw[rb][0], Pw[rb][1], Pw[rb][2], Pw[rb][3]}; return __builtin_bit_cast(s16x8, v); };

  TS(ts1);
  s16x8 vf[8], kf[2][4];
  auto do_kread = [&](auto KSC) {
    constexpr int KS = decltype(KSC)::value;
#pragma unroll
    for (int m = 0; m < 8; ++m) kf[m >> 2][m & 3] = lds_b128(kofs[m & 3] + KS * TILE + (m >> 2) * 4096);
    SB();
  };
  auto do_qk = [&](auto VSC, auto VRC) {
    constexpr int VS = decltype(VSC)::value; constexpr bool VR = decltype(VRC)::value != 0;
    sfor<8 * NRB>([&](auto M) {
      constexpr int m = decltype(M)::value, g8 = m / NRB, rb = m % NRB, kb = g8 >> 2, ks = g8 & 3, f = rb * 4 + ks;
      if constexpr (f < 8) { if constexpr (ks == 0) mfma_qk0<AQ(f)>(S[rb][kb], kf[kb][ks], NM[rb]); else mfma_qk<AQ(f)>(S[rb][kb], kf[kb][ks]); }
      else { if constexpr (ks == 0) mfma_v0(S[rb][kb], kf[kb][ks], Qv[f - 8], NM[rb]); else mfma_v(S[rb][kb], kf[kb][ks], Qv[f - 8]); }
      if constexpr (VR && rb == NRB - 1) { const unsigned a = vofs[g8] + VS * TILE; vf[g8] = cat8(lds_tr(a), lds_tr(a + 4096)); }
      SB();
    });
    if constexpr (NRB == 3) asm volatile("s_nop 7" : "+v"(S[0][0]), "+v"(S[0][1]), "+v"(S[1][0]), "+v"(S[1][1]), "+v"(S[2][0]), "+v"(S[2][1]));
    else asm volatile("s_nop 7" : "+v"(S[0][0]), "+v"(S[0][1]), "+v"(S[1][0]), "+v"(S[1][1]));
  };
  auto do_check = [&](bool first) {
    float ta = max2f(S[0][0][0], S[0][0][1]), tb = max2f(S[0][0][2], S[0][0][3]);
#pragma unroll
    for (int i = 1; i < 2 * NRB; ++i) { ta = max3f(ta, S[i >> 1][i & 1][0], S[i >> 1][i & 1][1]); tb = max3f(tb, S[i >> 1][i & 1][2], S[i >> 1][i & 1][3]); }
    if (__builtin_expect(first || __any(max2f(ta, tb) > THR), 0)) {
      sfor<NRB>([&](auto R) {
        constexpr int rb = decltype(R)::value;
        const float rm = xlane_max(rowmax(S[rb]));
        const float dl = first ? rm : fmaxf(rm, 0.f);
        const float al = first ? 1.f : __builtin_amdgcn_exp2f(-dl);
        S[rb][0] -= dl; S[rb][1] -= dl; NM[rb] -= dl; lsum[rb] *= al;
        if (!first) sfor<32>([&](auto E) { acc_scale<AO(rb, 0) + decltype(E)::value>(al); });
      });
    }
  };
  auto do_smpv = [&](auto NPC) {
    constexpr int NP = decltype(NPC)::value;
#pragma unroll
    for (int k = 0; k < 14; ++k) sm_item(0, k);
    asm volatile("s_nop 1" ::: "memory");
    SB();
    sfor<8 * NP>([&](auto M) {
      constexpr int m = decltype(M)::value, rb = m / 8, db = m % 8;
      mfma_acc<AO(rb, db)>(vf[db], Pf(rb));
      if constexpr (rb + 1 < NRB) {
        constexpr int k0 = db < 4 ? 2 * db : db < 6 ? 4 + db : 10 + 2 * (db - 6), k1 = db < 4 ? 2 * db + 2 : db < 6 ? 5 + db : 12 + 2 * (db - 6);
#pragma unroll
        for (int k = k0; k < k1; ++k) sm_item(rb + 1, k);
        if constexpr (db == 7) asm volatile("s_nop 1" ::: "memory");
      }
      SB();
    });
  };
#pragma unroll
  for (int rb = 0; rb < NRB; ++rb) { S[rb][0] = f32x4{-1e30f, -1e30f, -1e30f, -1e30f}; S[rb][1] = S[rb][0]; Pw[rb][0] = Pw[rb][1] = Pw[rb][2] = Pw[rb][3] = 0u; }
#pragma unroll
  for (int db = 0; db < 8; ++db) { vf[db] = s16x8{0, 0, 0, 0, 0, 0, 0, 0}; kf[db >> 2][db & 3] = vf[db]; }
  auto wait_vf = [&]() { asm volatile("s_waitcnt lgkmcnt(0)" : "+v"(vf[0]), "+v"(vf[1]), "+v"(vf[2]), "+v"(vf[3]), "+v"(vf[4]), "+v"(vf[5]), "+v"(vf[6]), "+v"(vf[7]) :: "memory"); };
  auto aiter = [&](int j, auto PHC) {
    constexpr int PH = decltype(PHC)::value;
    constexpr int KS = 2 * ((PH + 1) % 4), VS = KS + 1, KD = 2 * PH, VD = KD + 1;
    TS(tw0);
    if constexpr (SMALL) { if (j < 30) WAITV(16); else WAITV(0); }
    BARRIER();
    TS(tw1); twsum += tw1 - tw0;
    do_kread(IC<KS>{});
    if constexpr (SMALL) {
      if (j + 3 < 32) tileK(IC<KD>{}, kg + (size_t)(j + 3) * 8192);
      SB();
      do_smpv(IC<NRB>{});
      TS2(tw0); sA += tw0 - tw1;
      do_qk(IC<VS>{}, IC<1>{});
      TS2(tw1); sB += tw1 - tw0;
      do_check(j == 0);
      TS2(tw0); sC += tw0 - tw1;
      if (j + 3 < 32) tileV(IC<VD>{}, vg + (size_t)(j + 3) * 8192);
      wait_vf();
    } else {
      do_qk(IC<VS>{}, IC<1>{});
      TS2(tw0); sA += tw0 - tw1;
      do_check(j == 0);
      TS2(tw1); sB += tw1 - tw0;
      do_smpv(IC<NRB>{});
      TS2(tw0); sC += tw0 - tw1;
    }
  };
  for (int j = 0; j < 32; j += 4) { aiter(j, IC<0>{}); aiter(j + 1, IC<1>{}); aiter(j + 2, IC<2>{}); aiter(j + 3, IC<3>{}); }
  unsigned rp[2][8];
  {
    int l3 = lane; asm volatile("" : "+v"(l3));
    const unsigned ro0 = (4 * (l3 >> 4)) * 1024 + (l3 & 15) * 2, ro1 = ro0 + 16384;
    sfor<2>([&](auto F) { sfor<8>([&](auto J) {
      constexpr int f = decltype(F)::value, j = decltype(J)::value;
      rp[f][j] = gload_u16<(j & 3) * 1024 + f * 32>(j < 4 ? ro0 : ro1, wsrc);
    }); });
  }
  if constexpr (SMALL) do_smpv(IC<NRB>{});
  if (task == 0) {
    asm volatile("s_waitcnt lgkmcnt(0)" ::: "memory");
    BARRIER();
    xgw = xg1 + (size_t)bw * 40 * 1024; wqw = wqg1 + (size_t)hw * 8 * 8192; kg = kg1; vg = vg1; wsrc = wsrc1;
    prologue();
  }

  TS(ts3);
  asm volatile("s_nop 7\n\ts_nop 7" ::: "memory");
  sfor<NRB>([&](auto R) {
    constexpr int rb = decltype(R)::value;
    float l = lsum[rb]; l += __shfl_xor(l, 16); l += __shfl_xor(l, 32);
    const float il = __builtin_amdgcn_rcpf(l);
    sfor<4>([&](auto Tt) {
      constexpr int t = decltype(Tt)::value;
      float v[8];
      sfor<8>([&](auto E) { v[decltype(E)::value] = acc_rd<AO(rb, 2 * t) + decltype(E)::value>() * il; });
      u32x4 o = {cvtpk(v[0], v[1]), cvtpk(v[2], v[3]), cvtpk(v[4], v[5]), cvtpk(v[6], v[7])};
      *(u32x4*)(ob + (size_t)rb * (NH * 4 * 1024) + t * 1024) = o;
    });
  });
  asm volatile("s_waitcnt vmcnt(%c16)" : "+v"(rp[0][0]), "+v"(rp[0][1]), "+v"(rp[0][2]), "+v"(rp[0][3]), "+v"(rp[0][4]), "+v"(rp[0][5]), "+v"(rp[0][6]), "+v"(rp[0][7]),
               "+v"(rp[1][0]), "+v"(rp[1][1]), "+v"(rp[1][2]), "+v"(rp[1][3]), "+v"(rp[1][4]), "+v"(rp[1][5]), "+v"(rp[1][6]), "+v"(rp[1][7]) : "i"(NRB * 4) : "memory");
#pragma unroll
  for (int f = 0; f < 2; ++f) {
    u32x4 v = {rp[f][0] | (rp[f][1] << 16), rp[f][2] | (rp[f][3] << 16), rp[f][4] | (rp[f][5] << 16), rp[f][6] | (rp[f][7] << 16)};
    *(u32x4*)(wdst + f * 1024 + lane * 16) = v;
  }
  ob += (size_t)NB * 10 * NH * 4 * 1024; wdst += (size_t)8 * 128 * 4096;
  }
}

__global__ __launch_bounds__(512, 2)
void attn_kernel(const __hip_bfloat16* __restrict__ xA, const __hip_bfloat16* __restrict__ kA, const __hip_bfloat16* __restrict__ vA, const __hip_bfloat16* __restrict__ wqA,
                 const __hip_bfloat16* __restrict__ xB, const __hip_bfloat16* __restrict__ kB, const __hip_bfloat16* __restrict__ vB, const __hip_bfloat16* __restrict__ wqB,
                 const __hip_bfloat16* __restrict__ woA, const __hip_bfloat16* __restrict__ woB, __hip_bfloat16* __restrict__ ows) {
  __shared__ __attribute__((aligned(16))) char smem[20 * TILE];
  const unsigned lds0 = (unsigned)(size_t)smem;
  const int tid = threadIdx.x, lane = tid & 63;
  const int w = __builtin_amdgcn_readfirstlane(tid >> 6);
  const int e = w & 1, r = (w >> 1) & 1;
  const int idx = blockIdx.x, xcd = idx & 7, jj = idx >> 3;
  const int b = (xcd >> 2) * 8 + (jj >> 2), h = ((xcd & 3) * 4 + (jj & 3)) * 2 + e;
  const size_t xo = (size_t)b * SQ * DM * 2, wo_ = (size_t)h * DH * 2, ko = ((size_t)b * NH + h) * SK * DH * 2 + r * 4096;
  const unsigned ldsh = lds0 + e * (8 * TILE);
  const int rbg = w < 4 ? 3 * r : 6 + 2 * r;
  char* ob = (char*)ows + ((((size_t)b * 10 + rbg) * NH + h) * 4) * 1024 + lane * 16;
  const int rnt = idx & 7, rks = (idx >> 3) * 4 + (w >> 1);
  const size_t ro = (size_t)rks * 32 * DM + rnt * 64 + (w & 1) * 32;
  char* wdst = (char*)ows + WS_WO + ((size_t)rnt * 128 + rks) * 4096 + (w & 1) * 2048;
#define BODY_ARGS (const unsigned short*)woA + ro, (const unsigned short*)woB + ro, wdst, (const char*)xA + xo, (const char*)xB + xo, (const char*)wqA + wo_, (const char*)wqB + wo_, \
                  (const char*)kA + ko, (const char*)kB + ko, (const char*)vA + ko, (const char*)vB + ko, ob, lds0, ldsh, r, w & 3, 16 * rbg, lane
  if (w < 4) attn_body<3>(BODY_ARGS); else attn_body<2>(BODY_ARGS);
}

constexpr int K2_LDS = 160 * 1024;
__global__ __launch_bounds__(512, 2)
void oproj_kernel(const char* __restrict__ ws, __hip_bfloat16* __restrict__ out) {
  __shared__ __attribute__((aligned(16))) char smem[K2_LDS];
  const unsigned lds0 = (unsigned)(size_t)smem;
  const int tid = threadIdx.x, lane = tid & 63;
  const int w = __builtin_amdgcn_readfirstlane(tid >> 6);
  const int li = lane & 15, g = lane >> 4;
  const int idx = blockIdx.x, xcd = idx & 7, r = idx >> 3;
  const int nt = r & 7, bb = xcd * 4 + (r >> 3);
  const int blk = bb >> 4;
  const char* og = ws + (size_t)bb * 10 * 128 * 1024 + (size_t)w * 16 * 1024;
  const char* wg = ws + WS_WO + ((size_t)(blk * 8 + nt) * 128 + w * 16) * 4096;
  unsigned ovoff[10];
#pragma unroll
  for (int rb = 0; rb < 10; ++rb) ovoff[rb] = lane * 16 + rb * 131072;

  asm volatile("" ::: ACLOB);
  s16x8 of[10], wf[2][4];
  f32x4 av[2][4];
#pragma unroll
  for (int i = 0; i < 8; ++i) av[i >> 2][i & 3] = f32x4{0.f, 0.f, 0.f, 0.f};
  sfor<4>([&](auto I) { wf[0][decltype(I)::value] = gload16<decltype(I)::value * 1024>(ovoff[0], wg); });
  sfor<10>([&](auto R) { of[decltype(R)::value] = gload16<0>(ovoff[decltype(R)::value], og); });
  sfor<32>([&](auto I) { acc_zero4<4 * decltype(I)::value>(); });
  auto pin1 = [&](s16x8& v) { asm volatile("s_waitcnt vmcnt(13)" : "+v"(v) :: "memory"); };
  auto pin4 = [&](s16x8 (&v)[4]) { asm volatile("" : "+v"(v[0]), "+v"(v[1]), "+v"(v[2]), "+v"(v[3])); };
  auto step = [&](auto PHC, int ks) {
    constexpr int PH = decltype(PHC)::value;
    const int kn = ks + 1 < 16 ? ks + 1 : 15;
    const char* wgn = wg + (size_t)kn * 4096; const char* ogn = og + (size_t)kn * 1024;
    sfor<4>([&](auto I) { wf[PH ^ 1][decltype(I)::value] = gload16<decltype(I)::value * 1024>(ovoff[0], wgn); });
    sfor<10>([&](auto R) {
      constexpr int rb = decltype(R)::value;
      pin1(of[rb]);
      if constexpr (rb == 0) pin4(wf[PH]);
      SB();
      sfor<4>([&](auto NBc) { constexpr int nb = decltype(NBc)::value;
        if constexpr (rb < 8) mfma_acc<(rb * 4 + nb) * 4>(wf[PH][nb], of[rb]); else mfma_v(av[rb - 8][nb], wf[PH][nb], of[rb]); });
      of[rb] = gload16<0>(ovoff[rb], ogn);
      SB();
    });
  };
  for (int ks = 0; ks < 16; ks += 2) { step(IC<0>{}, ks); step(IC<1>{}, ks + 1); }
  asm volatile("s_waitcnt vmcnt(0)\n\ts_nop 7\n\ts_nop 7" : "+v"(av[0][0]), "+v"(av[0][1]), "+v"(av[0][2]), "+v"(av[0][3]), "+v"(av[1][0]), "+v"(av[1][1]), "+v"(av[1][2]), "+v"(av[1][3]) :: "memory");
  auto getacc = [&](auto RC, auto NC) { constexpr int rb = decltype(RC)::value, nb = decltype(NC)::value;
    if constexpr (rb < 8) return f32x4{acc_rd<(rb * 4 + nb) * 4>(), acc_rd<(rb * 4 + nb) * 4 + 1>(), acc_rd<(rb * 4 + nb) * 4 + 2>(), acc_rd<(rb * 4 + nb) * 4 + 3>()};
    else return av[rb - 8][nb]; };
  char* outb = (char*)out + (size_t)(bb & 15) * SQ * 2048 + blk * 1024 + nt * 128 + g * 8;
  sfor<2>([&](auto HC) {
    constexpr int hf = decltype(HC)::value;
    if constexpr (hf == 1) __syncthreads();
    sfor<20>([&](auto F) { constexpr int f = decltype(F)::value;
      *(lds_f32x4_ptr)(lds0 + (w * 20 + f) * 1024 + lane * 16) = getacc(IC<hf * 5 + f / 4>{}, IC<f % 4>{}); });
    __syncthreads();
#pragma unroll
    for (int i = 0; i < 3; ++i) {
      const int f = w + 8 * i;
      if (f < 20) {
        f32x4 sacc = *(lds_f32x4_ptr)(lds0 + f * 1024 + lane * 16);
#pragma unroll
        for (int sw = 1; sw < 8; ++sw) sacc += *(lds_f32x4_ptr)(lds0 + (sw * 20 + f) * 1024 + lane * 16);
        uint2 o2; o2.x = cvtpk(sacc[0], sacc[1]); o2.y = cvtpk(sacc[2], sacc[3]);
        *(uint2*)(outb + (size_t)(16 * (hf * 5 + (f >> 2)) + li) * 2048 + (f & 3) * 32) = o2;
      }
    }
  });
}

extern "C" void kernel_launch(void* const* d_in, const int* in_sizes, int n_in,
                              void* d_out, int out_size, void* d_ws, size_t ws_size,
                              hipStream_t stream) {
  const __hip_bfloat16* xA = (const __hip_bfloat16*)d_in[0];
  const __hip_bfloat16* kA = (const __hip_bfloat16*)d_in[1];
  const __hip_bfloat16* vA = (const __hip_bfloat16*)d_in[2];
  const __hip_bfloat16* wqA = (const __hip_bfloat16*)d_in[3];
  const __hip_bfloat16* woA = (const __hip_bfloat16*)d_in[4];
  const __hip_bfloat16* xB = (const __hip_bfloat16*)d_in[5];
  const __hip_bfloat16* kB = (const __hip_bfloat16*)d_in[6];
  const __hip_bfloat16* vB = (const __hip_bfloat16*)d_in[7];
  const __hip_bfloat16* wqB = (const __hip_bfloat16*)d_in[8];
  const __hip_bfloat16* woB = (const __hip_bfloat16*)d_in[9];
  __hip_bfloat16* ows = (__hip_bfloat16*)d_ws;
  attn_kernel<<<256, 512, 0, stream>>>(xA, kA, vA, wqA, xB, kB, vB, wqB, woA, woB, ows);
  oproj_kernel<<<256, 512, 0, stream>>>((const char*)d_ws, (__hip_bfloat16*)d_out);
}
```

```cpp
#include <hip/hip_runtime.h>
#include <hip/hip_bf16.h>
#include <utility>
#include <type_traits>

typedef __attribute__((ext_vector_type(8))) short s16x8;
typedef __attribute__((ext_vector_type(4))) short s16x4;
typedef __attribute__((ext_vector_type(4))) float f32x4;
typedef __attribute__((ext_vector_type(2))) float f32x2;
typedef __attribute__((ext_vector_type(2))) __bf16 bf16x2;
typedef __attribute__((ext_vector_type(4))) unsigned u32x4;
typedef __attribute__((address_space(3))) s16x4* lds_s16x4_ptr;
typedef __attribute__((address_space(3))) s16x8* lds_s16x8_ptr;
typedef __attribute__((address_space(3))) f32x4* lds_f32x4_ptr;

#define DI __device__ __forceinline__

constexpr int NB = 16, SQ = 160, DM = 512, NH = 32, SK = 1024, DH = 128;
constexpr float QSCALE = 0.088388347648318440f * 1.4426950408889634f;
constexpr size_t WS_WO = (size_t)48 << 20;
constexpr float THR = 8.0f;

DI unsigned cvtpk(float lo, float hi) { f32x2 v = {lo, hi}; bf16x2 b = __builtin_convertvector(v, bf16x2); return __builtin_bit_cast(unsigned, b); }
DI float max3f(float a, float b, float c) { float r; asm("v_max3_f32 %0, %1, %2, %3" : "=v"(r) : "v"(a), "v"(b), "v"(c)); return r; }
DI float max2f(float a, float b) { float r; asm("v_max_f32_e32 %0, %1, %2" : "=v"(r) : "v"(a), "v"(b)); return r; }
DI s16x8 lds_b128(unsigned addr) { return *(lds_s16x8_ptr)(addr); }
DI s16x4 lds_tr(unsigned addr) { return __builtin_amdgcn_ds_read_tr16_b64_v4i16((lds_s16x4_ptr)(addr)); }
DI s16x8 cat8(s16x4 a, s16x4 b) { return __builtin_shufflevector(a, b, 0, 1, 2, 3, 4, 5, 6, 7); }

template <int OFF> DI s16x8 gload16(unsigned voff, const void* gbase) {
  s16x8 r; asm volatile("global_load_dwordx4 %0, %1, %2 offset:%3" : "=v"(r) : "v"(voff), "s"(gbase), "i"(OFF) : "memory"); return r;
}
template <int OFF> DI unsigned gload_u16(unsigned voff, const void* gbase) {
  unsigned r; asm volatile("global_load_ushort %0, %1, %2 offset:%3" : "=v"(r) : "v"(voff), "s"(gbase), "i"(OFF) : "memory"); return r;
}
#define WAITV(n) asm volatile("s_waitcnt vmcnt(" #n ")" ::: "memory")
#define BARRIER() asm volatile("s_barrier" ::: "memory")

#define ACLOB "a0","a1","a2","a3","a4","a5","a6","a7","a8","a9","a10","a11","a12","a13","a14","a15","a16","a17","a18","a19","a20","a21","a22","a23","a24","a25","a26","a27","a28","a29","a30","a31","a32","a33","a34","a35","a36","a37","a38","a39","a40","a41","a42","a43","a44","a45","a46","a47","a48","a49","a50","a51","a52","a53","a54","a55","a56","a57","a58","a59","a60","a61","a62","a63","a64","a65","a66","a67","a68","a69","a70","a71","a72","a73","a74","a75","a76","a77","a78","a79","a80","a81","a82","a83","a84","a85","a86","a87","a88","a89","a90","a91","a92","a93","a94","a95","a96","a97","a98","a99","a100","a101","a102","a103","a104","a105","a106","a107","a108","a109","a110","a111","a112","a113","a114","a115","a116","a117","a118","a119","a120","a121","a122","a123","a124","a125","a126","a127"
#define SB() __builtin_amdgcn_sched_barrier(0)
template <int... I, class F> DI void sfor_impl(std::integer_sequence<int, I...>, F&& f) { (f(std::integral_constant<int, I>{}), ...); }
template <int N, class F> DI void sfor(F&& f) { sfor_impl(std::make_integer_sequence<int, N>{}, f); }
template <int N> using IC = std::integral_constant<int, N>;
constexpr int AO(int rb, int db) { return (rb * 8 + db) * 4; }
constexpr int AQ(int f) { return 96 + f * 4; }
template <int A> DI void mfma_acc(s16x8 a, s16x8 b) {
  asm volatile("v_mfma_f32_16x16x32_bf16 a[%c2:%c3], %0, %1, a[%c2:%c3]" :: "v"(a), "v"(b), "i"(A), "i"(A + 3));
}
template <int A> DI void mfma_qk0(f32x4& S, s16x8 kf, const f32x4& nm) {
  asm volatile("v_mfma_f32_16x16x32_bf16 %0, %1, a[%c3:%c4], %2" : "=&v"(S) : "v"(kf), "v"(nm), "i"(A), "i"(A + 3));
}
template <int A> DI void mfma_qk(f32x4& S, s16x8 kf) {
  asm volatile("v_mfma_f32_16x16x32_bf16 %0, %1, a[%c2:%c3], %0" : "+v"(S) : "v"(kf), "i"(A), "i"(A + 3));
}
DI void mfma_v0(f32x4& D, s16x8 a, s16x8 b, const f32x4& c) { asm volatile("v_mfma_f32_16x16x32_bf16 %0, %1, %2, %3" : "=&v"(D) : "v"(a), "v"(b), "v"(c)); }
DI void mfma_v(f32x4& D, s16x8 a, s16x8 b) { asm volatile("v_mfma_f32_16x16x32_bf16 %0, %1, %2, %0" : "+v"(D) : "v"(a), "v"(b)); }
template <int A> DI float acc_rd() { float r; asm volatile("v_accvgpr_read_b32 %0, a[%c1]" : "=v"(r) : "i"(A)); return r; }
template <int A> DI void acc_wr(unsigned v) { asm volatile("v_accvgpr_write_b32 a[%c1], %0" :: "v"(v), "i"(A)); }
template <int A> DI void acc_zero4() { asm volatile("v_accvgpr_write_b32 a[%c0], 0\n\tv_accvgpr_write_b32 a[%c1], 0\n\tv_accvgpr_write_b32 a[%c2], 0\n\tv_accvgpr_write_b32 a[%c3], 0" :: "i"(A), "i"(A + 1), "i"(A + 2), "i"(A + 3)); }
template <int A> DI void acc_scale(float f) { float t; asm volatile("v_accvgpr_read_b32 %0, a[%c2]\n\tv_mul_f32 %0, %0, %1\n\tv_accvgpr_write_b32 a[%c2], %0" : "=&v"(t) : "v"(f), "i"(A)); }
DI void add4(float& l, const f32x4& v) { asm volatile("v_add_f32_e32 %0, %0, %1\n\tv_add_f32_e32 %0, %0, %2\n\tv_add_f32_e32 %0, %0, %3\n\tv_add_f32_e32 %0, %0, %4" : "+v"(l) : "v"(v[0]), "v"(v[1]), "v"(v[2]), "v"(v[3])); }
template <int LOFF> DI void dma4_nt(unsigned ldsw, unsigned v0, unsigned v1, unsigned v2, unsigned v3, const void* gbase) {
  asm volatile("s_add_u32 m0, %0, %6\n\ts_nop 0\n\t"
               "global_load_lds_dwordx4 %1, %5 nt\n\t"
               "global_load_lds_dwordx4 %2, %5 offset:1024 nt\n\t"
               "global_load_lds_dwordx4 %3, %5 offset:2048 nt\n\t"
               "global_load_lds_dwordx4 %4, %5 offset:3072 nt"
               :: "s"(ldsw), "v"(v0), "v"(v1), "v"(v2), "v"(v3), "s"(gbase), "i"(LOFF) : "memory", "scc");
}
template <int LOFF> DI void dma_m0(unsigned ldsw, unsigned v0, const void* gbase) {
  asm volatile("s_add_u32 m0, %0, %3\n\ts_nop 0\n\tglobal_load_lds_dwordx4 %1, %2" :: "s"(ldsw), "v"(v0), "s"(gbase), "i"(LOFF) : "memory", "scc");
}
constexpr int TILE = 8192;
constexpr int XBUF(int c) { return (c % 3) == 0 ? 40 * 1024 : (c % 3) == 1 ? 140 * 1024 : 104 * 1024; }

template <int NRB>
DI void attn_body(const unsigned short* wsrc, const unsigned short* wsrc1, char* wdst, const char* xg, const char* xg1, const char* wqg, const char* wqg1, const char* kg, const char* kg1, const char* vg, const char* vg1, char* ob, const unsigned lds0, const unsigned ldsh, const int r, const int bw, const int row0, const int lane
                  ) {
  constexpr bool SMALL = NRB == 2;
  const unsigned ldsw = ldsh + r * 4096;
  const int hw = SMALL ? 2 + r : r;
  const unsigned ldsq = ldsh + hw * 2048;
  const char* wqw = wqg + (size_t)hw * 8 * 8192;
  unsigned voffK[4], voffV[2], voffW, wofs[4], voffX[2], xofs[2];
  auto addr_dma_q = [&]() {
    int l = lane; asm volatile("" : "+v"(l)); const int li = l & 15, g = l >> 4;
    voffW = g * 8192 + ((li ^ ((g & 1) | ((g >> 1) << 2) | (r << 3))) * 16);
#pragma unroll
    for (int n = 0; n < 2; ++n) voffX[n] = (l >> 3) * 1024 + (((l & 7) ^ (4 * ((n + bw) & 1) + g)) * 16);
  };
  auto addr_q = [&]() {
    int l = lane; asm volatile("" : "+v"(l)); const int li = l & 15, g = l >> 4, q4 = (l >> 2) & 3, p4 = l & 3;
#pragma unroll
    for (int n = 0; n < 4; ++n) voffK[n] = g * 256 + (((li ^ g) ^ (4 * n)) * 16);
#pragma unroll
    for (int n = 0; n < 2; ++n) voffV[n] = g * 256 + ((li ^ (g << 1) ^ (n << 3)) * 16);
#pragma unroll
    for (int t = 0; t < 4; ++t) wofs[t] = ldsh + (8 * g + q4) * 256 + (((4 * t + p4) ^ ((q4 & 1) | ((q4 >> 1) << 2) | ((g & 1) << 3))) * 16) + (p4 & 1) * 8;
#pragma unroll
    for (int kk = 0; kk < 2; ++kk) xofs[kk] = lds0 + (row0 + li) * 128 + (((4 * kk + g) ^ (li >> 1)) * 16);
  };

  auto pieceW = [&](auto N, auto I) { constexpr int n = decltype(N)::value, i = decltype(I)::value;
    dma_m0<(n % 5) * TILE + i * 1024>(ldsq, voffW, wqw + (size_t)(32 * n) * 8192 + (size_t)i * 32768); };
  auto tileW = [&](auto N) { pieceW(N, IC<0>{}); pieceW(N, IC<1>{}); };
  auto tileK = [&](auto SL, const char* gb) { dma4_nt<decltype(SL)::value * TILE>(ldsw, voffK[0], voffK[1], voffK[2], voffK[3], gb); };
  auto tileV = [&](auto SL, const char* gb) { dma4_nt<decltype(SL)::value * TILE>(ldsw, voffV[0], voffV[1], voffV[0], voffV[1], gb); };

  const unsigned ldsx = lds0 + bw * 5120;
  const char* xgw = xg + (size_t)bw * 40 * 1024;
  auto pieceX = [&](auto C, auto PC) { constexpr int c = decltype(C)::value, pc = decltype(PC)::value;
    dma_m0<XBUF(c) + pc * 1024>(ldsx, voffX[pc & 1], xgw + (size_t)pc * 8192 + c * 128); };
  auto chunkX = [&](auto C) { sfor<5>([&](auto PC) { pieceX(C, PC); }); };
  auto prologue = [&]() {
    addr_dma_q();
    if constexpr (!SMALL) chunkX(IC<0>{});
    tileW(IC<0>{}); tileW(IC<1>{});
    if constexpr (!SMALL) chunkX(IC<1>{});
    tileW(IC<2>{});
    if constexpr (!SMALL) chunkX(IC<2>{});
    tileW(IC<3>{}); tileW(IC<4>{});
  };
  prologue();
  for (int task = 0; task < 2; ++task) {
  addr_q();
  asm volatile("" ::: ACLOB);
  sfor<NRB * 8>([&](auto I) { acc_zero4<4 * decltype(I)::value>(); });
  s16x8 wf[2][8], xf[2][NRB];
  auto wread = [&](auto N, auto I) { constexpr int n = decltype(N)::value, i = decltype(I)::value;
    const unsigned a = (wofs[i >> 1] ^ ((i & 1) * 8)) + (n % 5) * TILE; wf[n & 1][i] = cat8(lds_tr(a), lds_tr(a + 1024)); };
  auto xread = [&](auto N, auto R) { constexpr int n = decltype(N)::value, rb = decltype(R)::value;
    xf[n & 1][rb] = lds_b128(xofs[n & 1] + XBUF(n >> 1) + rb * 2048); };
  if constexpr (SMALL) WAITV(8); else WAITV(18);
  BARRIER();

  sfor<NRB>([&](auto R) { xread(IC<0>{}, R); });
  sfor<8>([&](auto I) { wread(IC<0>{}, I); });
  auto qstep = [&](auto N, auto WSC) {
    constexpr int n = decltype(N)::value;
    constexpr int WN = SMALL ? decltype(WSC)::value : (n == 0 ? 16 : n <= 3 ? 11 : n <= 11 ? ((n & 1) ? 11 : 16) : n == 12 ? 9 : n == 13 ? 2 : n == 14 ? 0 : 63);

    asm volatile("s_waitcnt vmcnt(%0) lgkmcnt(0)" :: "i"(WN) : "memory");
    BARRIER();

    SB();
    constexpr bool hasW = n + 5 < 16, hasX = !SMALL && (n & 1) == 1 && (n + 5) / 2 < 8, hasKV = SMALL && n >= 12;
    sfor<8 * NRB>([&](auto M) { constexpr int m = decltype(M)::value, d = m / NRB, rb = m % NRB;
      mfma_acc<AO(rb, d)>(wf[n & 1][d], xf[n & 1][rb]);
      if constexpr (n < 15) {
        if constexpr (m < NRB) xread(IC<n + 1>{}, IC<m>{});
        else if constexpr (m < NRB + 8) wread(IC<n + 1>{}, IC<m - NRB>{});
      }
      if constexpr (hasW && (m == 1 || m == 3)) pieceW(IC<hasW ? n + 5 : 0>{}, IC<m / 2>{});
      if constexpr (hasX && m >= 5 && m <= 13 && (m & 1)) pieceX(IC<hasX ? (n + 5) / 2 : 0>{}, IC<(m - 5) / 2>{});
      if constexpr (hasKV && m == 1) {
        if constexpr (n == 12) tileK(IC<2>{}, kg);
        if constexpr (n == 13) tileV(IC<3>{}, vg);
        if constexpr (n == 14) { tileK(IC<4>{}, kg + 8192); tileV(IC<5>{}, vg + 8192); }
        if constexpr (n == 15) { tileK(IC<6>{}, kg + 16384); tileV(IC<7>{}, vg + 16384); }
      }
      SB();
    });
  };
  qstep(IC<0>{}, IC<6>{}); qstep(IC<1>{}, IC<6>{}); qstep(IC<2>{}, IC<6>{}); qstep(IC<3>{}, IC<6>{});
  qstep(IC<4>{}, IC<6>{}); qstep(IC<5>{}, IC<6>{}); qstep(IC<6>{}, IC<6>{}); qstep(IC<7>{}, IC<6>{});
  qstep(IC<8>{}, IC<6>{}); qstep(IC<9>{}, IC<6>{}); qstep(IC<10>{}, IC<6>{}); qstep(IC<11>{}, IC<6>{});
  qstep(IC<12>{}, IC<4>{}); qstep(IC<13>{}, IC<6>{}); qstep(IC<14>{}, IC<8>{}); qstep(IC<15>{}, IC<63>{});

  asm volatile("s_nop 7\n\ts_nop 7" ::: "memory");
  s16x8 Qv[4];
  sfor<NRB>([&](auto R) { sfor<4>([&](auto Tt) {
    constexpr int rb = decltype(R)::value, t = decltype(Tt)::value, f = rb * 4 + t;
    float v[8];
    sfor<8>([&](auto E) { v[decltype(E)::value] = acc_rd<AO(rb, 2 * t) + decltype(E)::value>() * QSCALE; });
    const unsigned c0 = cvtpk(v[0], v[1]), c1 = cvtpk(v[2], v[3]), c2 = cvtpk(v[4], v[5]), c3 = cvtpk(v[6], v[7]);
    const bool odd = (lane & 16) != 0;
    const unsigned q0 = odd ? c2 : c0, q1 = odd ? c3 : c1, q2 = odd ? c0 : c2, q3 = odd ? c1 : c3;
    if constexpr (f < 8) { acc_wr<AQ(f)>(q0); acc_wr<AQ(f) + 1>(q1); acc_wr<AQ(f) + 2>(q2); acc_wr<AQ(f) + 3>(q3); }
    else { u32x4 q = {q0, q1, q2, q3}; Qv[f - 8] = __builtin_bit_cast(s16x8, q); }
  }); });
  sfor<NRB * 8>([&](auto I) { acc_zero4<4 * decltype(I)::value>(); });

  unsigned kofs[4], vofs[8];
  {
    int l2 = lane; asm volatile("" : "+v"(l2));
    const int li2 = l2 & 15, g2 = l2 >> 4, q2 = (l2 >> 2) & 3, p2 = l2 & 3;
#pragma unroll
    for (int ks = 0; ks < 4; ++ks) kofs[ks] = ldsh + li2 * 256 + (((4 * ks + g2) ^ li2) * 16);
    const unsigned r7 = 4 * (g2 & 1) + q2;
#pragma unroll
    for (int db = 0; db < 8; ++db) vofs[db] = ldsh + (4 * g2 + q2) * 256 + (r7 ^ db) * 32 + (p2 >> 1) * 16 + (p2 & 1) * 8;
  }
  f32x4 NM[NRB], S[NRB][2];
  float lsum[NRB];
  unsigned Pw[NRB][4];
#pragma unroll
  for (int rb = 0; rb < NRB; ++rb) { lsum[rb] = 0.f; NM[rb] = f32x4{0.f, 0.f, 0.f, 0.f}; }
  auto rowmax = [&](const f32x4 (&Sr)[2]) {
    float t = max3f(Sr[0][0], Sr[0][1], Sr[0][2]);
    t = max3f(t, Sr[0][3], Sr[1][0]); t = max3f(t, Sr[1][1], Sr[1][2]); t = max2f(t, Sr[1][3]);
    return t;
  };
  auto xlane_max = [&](float t) { t = fmaxf(t, __shfl_xor(t, 16)); t = fmaxf(t, __shfl_xor(t, 32)); return t; };
  auto sm_item = [&](int rb, int k) {
    if (k < 8) { float t = __builtin_amdgcn_exp2f(S[rb][k >> 2][k & 3]); asm volatile("" : "+v"(t)); S[rb][k >> 2][k & 3] = t; }
    else if (k < 10) add4(lsum[rb], S[rb][k - 8]);
    else { const int c = k - 10; unsigned t = cvtpk(S[rb][c >> 1][(c & 1) * 2], S[rb][c >> 1][(c & 1) * 2 + 1]); asm volatile("" : "+v"(t)); Pw[rb][c] = t; }
  };
  auto Pf = [&](int rb) { u32x4 v = {Pw[rb][0], Pw[rb][1], Pw[rb][2], Pw[rb][3]}; return __builtin_bit_cast(s16x8, v); };

  s16x8 vf[8], kf[2][4];
  auto do_kread = [&](auto KSC) {
    constexpr int KS = decltype(KSC)::value;
#pragma unroll
    for (int m = 0; m < 8; ++m) kf[m >> 2][m & 3] = lds_b128(kofs[m & 3] + KS * TILE + (m >> 2) * 4096);
    SB();
  };
  auto do_qk = [&](auto VSC, auto VRC) {
    constexpr int VS = decltype(VSC)::value; constexpr bool VR = decltype(VRC)::value != 0;
    sfor<8 * NRB>([&](auto M) {
      constexpr int m = decltype(M)::value, g8 = m / NRB, rb = m % NRB, kb = g8 >> 2, ks = g8 & 3, f = rb * 4 + ks;
      if constexpr (f < 8) { if constexpr (ks == 0) mfma_qk0<AQ(f)>(S[rb][kb], kf[kb][ks], NM[rb]); else mfma_qk<AQ(f)>(S[rb][kb], kf[kb][ks]); }
      else { if constexpr (ks == 0) mfma_v0(S[rb][kb], kf[kb][ks], Qv[f - 8], NM[rb]); else mfma_v(S[rb][kb], kf[kb][ks], Qv[f - 8]); }
      if constexpr (VR && rb == NRB - 1) { const unsigned a = vofs[g8] + VS * TILE; vf[g8] = cat8(lds_tr(a), lds_tr(a + 4096)); }
      SB();
    });
    if constexpr (NRB == 3) asm volatile("s_nop 7" : "+v"(S[0][0]), "+v"(S[0][1]), "+v"(S[1][0]), "+v"(S[1][1]), "+v"(S[2][0]), "+v"(S[2][1]));
    else asm volatile("s_nop 7" : "+v"(S[0][0]), "+v"(S[0][1]), "+v"(S[1][0]), "+v"(S[1][1]));
  };
  auto do_check = [&](bool first) {
    float ta = max2f(S[0][0][0], S[0][0][1]), tb = max2f(S[0][0][2], S[0][0][3]);
#pragma unroll
    for (int i = 1; i < 2 * NRB; ++i) { ta = max3f(ta, S[i >> 1][i & 1][0], S[i >> 1][i & 1][1]); tb = max3f(tb, S[i >> 1][i & 1][2], S[i >> 1][i & 1][3]); }
    if (__builtin_expect(first || __any(max2f(ta, tb) > THR), 0)) {
      sfor<NRB>([&](auto R) {
        constexpr int rb = decltype(R)::value;
        const float rm = xlane_max(rowmax(S[rb]));
        const float dl = first ? rm : fmaxf(rm, 0.f);
        const float al = first ? 1.f : __builtin_amdgcn_exp2f(-dl);
        S[rb][0] -= dl; S[rb][1] -= dl; NM[rb] -= dl; lsum[rb] *= al;
        if (!first) sfor<32>([&](auto E) { acc_scale<AO(rb, 0) + decltype(E)::value>(al); });
      });
    }
  };
  auto do_smpv = [&](auto NPC) {
    constexpr int NP = decltype(NPC)::value;
#pragma unroll
    for (int k = 0; k < 14; ++k) sm_item(0, k);
    asm volatile("s_nop 1" ::: "memory");
    SB();
    sfor<8 * NP>([&](auto M) {
      constexpr int m = decltype(M)::value, rb = m / 8, db = m % 8;
      mfma_acc<AO(rb, db)>(vf[db], Pf(rb));
      if constexpr (rb + 1 < NRB) {
        constexpr int k0 = db < 4 ? 2 * db : db < 6 ? 4 + db : 10 + 2 * (db - 6), k1 = db < 4 ? 2 * db + 2 : db < 6 ? 5 + db : 12 + 2 * (db - 6);
#pragma unroll
        for (int k = k0; k < k1; ++k) sm_item(rb + 1, k);
        if constexpr (db == 7) asm volatile("s_nop 1" ::: "memory");
      }
      SB();
    });
  };
#pragma unroll
  for (int rb = 0; rb < NRB; ++rb) { S[rb][0] = f32x4{-1e30f, -1e30f, -1e30f, -1e30f}; S[rb][1] = S[rb][0]; Pw[rb][0] = Pw[rb][1] = Pw[rb][2] = Pw[rb][3] = 0u; }
#pragma unroll
  for (int db = 0; db < 8; ++db) { vf[db] = s16x8{0, 0, 0, 0, 0, 0, 0, 0}; kf[db >> 2][db & 3] = vf[db]; }
  auto wait_vf = [&]() { asm volatile("s_waitcnt lgkmcnt(0)" : "+v"(vf[0]), "+v"(vf[1]), "+v"(vf[2]), "+v"(vf[3]), "+v"(vf[4]), "+v"(vf[5]), "+v"(vf[6]), "+v"(vf[7]) :: "memory"); };
  auto aiter = [&](int j, auto PHC) {
    constexpr int PH = decltype(PHC)::value;
    constexpr int KS = 2 * ((PH + 1) % 4), VS = KS + 1, KD = 2 * PH, VD = KD + 1;

    if constexpr (SMALL) { if (j < 30) WAITV(16); else WAITV(0); }
    BARRIER();

    do_kread(IC<KS>{});
    if constexpr (SMALL) {
      if (j + 3 < 32) tileK(IC<KD>{}, kg + (size_t)(j + 3) * 8192);
      SB();
      do_smpv(IC<NRB>{});

      do_qk(IC<VS>{}, IC<1>{});

      do_check(j == 0);

      if (j + 3 < 32) tileV(IC<VD>{}, vg + (size_t)(j + 3) * 8192);
      wait_vf();
    } else {
      do_qk(IC<VS>{}, IC<1>{});

      do_check(j == 0);

      do_smpv(IC<NRB>{});

    }
  };
  for (int j = 0; j < 32; j += 4) { aiter(j, IC<0>{}); aiter(j + 1, IC<1>{}); aiter(j + 2, IC<2>{}); aiter(j + 3, IC<3>{}); }
  unsigned rp[2][8];
  {
    int l3 = lane; asm volatile("" : "+v"(l3));
    const unsigned ro0 = (4 * (l3 >> 4)) * 1024 + (l3 & 15) * 2, ro1 = ro0 + 16384;
    sfor<2>([&](auto F) { sfor<8>([&](auto J) {
      constexpr int f = decltype(F)::value, j = decltype(J)::value;
      rp[f][j] = gload_u16<(j & 3) * 1024 + f * 32>(j < 4 ? ro0 : ro1, wsrc);
    }); });
  }
  if constexpr (SMALL) do_smpv(IC<NRB>{});
  if (task == 0) {
    asm volatile("s_waitcnt lgkmcnt(0)" ::: "memory");
    BARRIER();
    xgw = xg1 + (size_t)bw * 40 * 1024; wqw = wqg1 + (size_t)hw * 8 * 8192; kg = kg1; vg = vg1; wsrc = wsrc1;
    prologue();
  }

  asm volatile("s_nop 7\n\ts_nop 7" ::: "memory");
  sfor<NRB>([&](auto R) {
    constexpr int rb = decltype(R)::value;
    float l = lsum[rb]; l += __shfl_xor(l, 16); l += __shfl_xor(l, 32);
    const float il = __builtin_amdgcn_rcpf(l);
    sfor<4>([&](auto Tt) {
      constexpr int t = decltype(Tt)::value;
      float v[8];
      sfor<8>([&](auto E) { v[decltype(E)::value] = acc_rd<AO(rb, 2 * t) + decltype(E)::value>() * il; });
      u32x4 o = {cvtpk(v[0], v[1]), cvtpk(v[2], v[3]), cvtpk(v[4], v[5]), cvtpk(v[6], v[7])};
      *(u32x4*)(ob + (size_t)rb * (NH * 4 * 1024) + t * 1024) = o;
    });
  });
  asm volatile("s_waitcnt vmcnt(%c16)" : "+v"(rp[0][0]), "+v"(rp[0][1]), "+v"(rp[0][2]), "+v"(rp[0][3]), "+v"(rp[0][4]), "+v"(rp[0][5]), "+v"(rp[0][6]), "+v"(rp[0][7]),
               "+v"(rp[1][0]), "+v"(rp[1][1]), "+v"(rp[1][2]), "+v"(rp[1][3]), "+v"(rp[1][4]), "+v"(rp[1][5]), "+v"(rp[1][6]), "+v"(rp[1][7]) : "i"(NRB * 4) : "memory");
#pragma unroll
  for (int f = 0; f < 2; ++f) {
    u32x4 v = {rp[f][0] | (rp[f][1] << 16), rp[f][2] | (rp[f][3] << 16), rp[f][4] | (rp[f][5] << 16), rp[f][6] | (rp[f][7] << 16)};
    *(u32x4*)(wdst + f * 1024 + lane * 16) = v;
  }
  ob += (size_t)NB * 10 * NH * 4 * 1024; wdst += (size_t)8 * 128 * 4096;
  }
}

__global__ __launch_bounds__(512, 2)
void attn_kernel(const __hip_bfloat16* __restrict__ xA, const __hip_bfloat16* __restrict__ kA, const __hip_bfloat16* __restrict__ vA, const __hip_bfloat16* __restrict__ wqA,
                 const __hip_bfloat16* __restrict__ xB, const __hip_bfloat16* __restrict__ kB, const __hip_bfloat16* __restrict__ vB, const __hip_bfloat16* __restrict__ wqB,
                 const __hip_bfloat16* __restrict__ woA, const __hip_bfloat16* __restrict__ woB, __hip_bfloat16* __restrict__ ows) {
  __shared__ __attribute__((aligned(16))) char smem[20 * TILE];
  const unsigned lds0 = (unsigned)(size_t)smem;
  const int tid = threadIdx.x, lane = tid & 63;
  const int w = __builtin_amdgcn_readfirstlane(tid >> 6);
  const int e = w & 1, r = (w >> 1) & 1;
  const int idx = blockIdx.x, xcd = idx & 7, jj = idx >> 3;
  const int b = (xcd >> 2) * 8 + (jj >> 2), h = ((xcd & 3) * 4 + (jj & 3)) * 2 + e;
  const size_t xo = (size_t)b * SQ * DM * 2, wo_ = (size_t)h * DH * 2, ko = ((size_t)b * NH + h) * SK * DH * 2 + r * 4096;
  const unsigned ldsh = lds0 + e * (8 * TILE);
  const int rbg = w < 4 ? 3 * r : 6 + 2 * r;
  char* ob = (char*)ows + ((((size_t)b * 10 + rbg) * NH + h) * 4) * 1024 + lane * 16;
  const int rnt = idx & 7, rks = (idx >> 3) * 4 + (w >> 1);
  const size_t ro = (size_t)rks * 32 * DM + rnt * 64 + (w & 1) * 32;
  char* wdst = (char*)ows + WS_WO + ((size_t)rnt * 128 + rks) * 4096 + (w & 1) * 2048;
#define BODY_ARGS (const unsigned short*)woA + ro, (const unsigned short*)woB + ro, wdst, (const char*)xA + xo, (const char*)xB + xo, (const char*)wqA + wo_, (const char*)wqB + wo_, \
                  (const char*)kA + ko, (const char*)kB + ko, (const char*)vA + ko, (const char*)vB + ko, ob, lds0, ldsh, r, w & 3, 16 * rbg, lane
  if (w < 4) attn_body<3>(BODY_ARGS); else attn_body<2>(BODY_ARGS);
}

constexpr int K2_LDS = 160 * 1024;
__global__ __launch_bounds__(512, 2)
void oproj_kernel(const char* __restrict__ ws, __hip_bfloat16* __restrict__ out) {
  __shared__ __attribute__((aligned(16))) char smem[K2_LDS];
  const unsigned lds0 = (unsigned)(size_t)smem;
  const int tid = threadIdx.x, lane = tid & 63;
  const int w = __builtin_amdgcn_readfirstlane(tid >> 6);
  const int li = lane & 15, g = lane >> 4;
  const int idx = blockIdx.x, xcd = idx & 7, r = idx >> 3;
  const int nt = r & 7, bb = xcd * 4 + (r >> 3);
  const int blk = bb >> 4;
  const char* og = ws + (size_t)bb * 10 * 128 * 1024 + (size_t)w * 16 * 1024;
  const char* wg = ws + WS_WO + ((size_t)(blk * 8 + nt) * 128 + w * 16) * 4096;
  unsigned ovoff[10];
#pragma unroll
  for (int rb = 0; rb < 10; ++rb) ovoff[rb] = lane * 16 + rb * 131072;

  asm volatile("" ::: ACLOB);
  s16x8 of[10], wf[2][4];
  f32x4 av[2][4];
#pragma unroll
  for (int i = 0; i < 8; ++i) av[i >> 2][i & 3] = f32x4{0.f, 0.f, 0.f, 0.f};
  sfor<4>([&](auto I) { wf[0][decltype(I)::value] = gload16<decltype(I)::value * 1024>(ovoff[0], wg); });
  sfor<10>([&](auto R) { of[decltype(R)::value] = gload16<0>(ovoff[decltype(R)::value], og); });
  sfor<32>([&](auto I) { acc_zero4<4 * decltype(I)::value>(); });
  auto pin1 = [&](s16x8& v) { asm volatile("s_waitcnt vmcnt(13)" : "+v"(v) :: "memory"); };
  auto pin4 = [&](s16x8 (&v)[4]) { asm volatile("" : "+v"(v[0]), "+v"(v[1]), "+v"(v[2]), "+v"(v[3])); };
  auto step = [&](auto PHC, int ks) {
    constexpr int PH = decltype(PHC)::value;
    const int kn = ks + 1 < 16 ? ks + 1 : 15;
    const char* wgn = wg + (size_t)kn * 4096; const char* ogn = og + (size_t)kn * 1024;
    sfor<4>([&](auto I) { wf[PH ^ 1][decltype(I)::value] = gload16<decltype(I)::value * 1024>(ovoff[0], wgn); });
    sfor<10>([&](auto R) {
      constexpr int rb = decltype(R)::value;
      pin1(of[rb]);
      if constexpr (rb == 0) pin4(wf[PH]);
      SB();
      sfor<4>([&](auto NBc) { constexpr int nb = decltype(NBc)::value;
        if constexpr (rb < 8) mfma_acc<(rb * 4 + nb) * 4>(wf[PH][nb], of[rb]); else mfma_v(av[rb - 8][nb], wf[PH][nb], of[rb]); });
      of[rb] = gload16<0>(ovoff[rb], ogn);
      SB();
    });
  };
  for (int ks = 0; ks < 16; ks += 2) { step(IC<0>{}, ks); step(IC<1>{}, ks + 1); }
  asm volatile("s_waitcnt vmcnt(0)\n\ts_nop 7\n\ts_nop 7" : "+v"(av[0][0]), "+v"(av[0][1]), "+v"(av[0][2]), "+v"(av[0][3]), "+v"(av[1][0]), "+v"(av[1][1]), "+v"(av[1][2]), "+v"(av[1][3]) :: "memory");
  auto getacc = [&](auto RC, auto NC) { constexpr int rb = decltype(RC)::value, nb = decltype(NC)::value;
    if constexpr (rb < 8) return f32x4{acc_rd<(rb * 4 + nb) * 4>(), acc_rd<(rb * 4 + nb) * 4 + 1>(), acc_rd<(rb * 4 + nb) * 4 + 2>(), acc_rd<(rb * 4 + nb) * 4 + 3>()};
    else return av[rb - 8][nb]; };
  char* outb = (char*)out + (size_t)(bb & 15) * SQ * 2048 + blk * 1024 + nt * 128 + g * 8;
  sfor<2>([&](auto HC) {
    constexpr int hf = decltype(HC)::value;
    if constexpr (hf == 1) __syncthreads();
    sfor<20>([&](auto F) { constexpr int f = decltype(F)::value;
      *(lds_f32x4_ptr)(lds0 + (w * 20 + f) * 1024 + lane * 16) = getacc(IC<hf * 5 + f / 4>{}, IC<f % 4>{}); });
    __syncthreads();
#pragma unroll
    for (int i = 0; i < 3; ++i) {
      const int f = w + 8 * i;
      if (f < 20) {
        f32x4 sacc = *(lds_f32x4_ptr)(lds0 + f * 1024 + lane * 16);
#pragma unroll
        for (int sw = 1; sw < 8; ++sw) sacc += *(lds_f32x4_ptr)(lds0 + (sw * 20 + f) * 1024 + lane * 16);
        uint2 o2; o2.x = cvtpk(sacc[0], sacc[1]); o2.y = cvtpk(sacc[2], sacc[3]);
        *(uint2*)(outb + (size_t)(16 * (hf * 5 + (f >> 2)) + li) * 2048 + (f & 3) * 32) = o2;
      }
    }
  });
}

extern "C" void kernel_launch(void* const* d_in, const int* in_sizes, int n_in,
                              void* d_out, int out_size, void* d_ws, size_t ws_size,
                              hipStream_t stream) {
  const __hip_bfloat16* xA = (const __hip_bfloat16*)d_in[0];
  const __hip_bfloat16* kA = (const __hip_bfloat16*)d_in[1];
  const __hip_bfloat16* vA = (const __hip_bfloat16*)d_in[2];
  const __hip_bfloat16* wqA = (const __hip_bfloat16*)d_in[3];
  const __hip_bfloat16* woA = (const __hip_bfloat16*)d_in[4];
  const __hip_bfloat16* xB = (const __hip_bfloat16*)d_in[5];
  const __hip_bfloat16* kB = (const __hip_bfloat16*)d_in[6];
  const __hip_bfloat16* vB = (const __hip_bfloat16*)d_in[7];
  const __hip_bfloat16* wqB = (const __hip_bfloat16*)d_in[8];
  const __hip_bfloat16* woB = (const __hip_bfloat16*)d_in[9];
  __hip_bfloat16* ows = (__hip_bfloat16*)d_ws;
  attn_kernel<<<256, 512, 0, stream>>>(xA, kA, vA, wqA, xB, kB, vB, wqB, woA, woB, ows);
  oproj_kernel<<<256, 512, 0, stream>>>((const char*)d_ws, (__hip_bfloat16*)d_out);
}
```
